```python
import torch

def _softmax(x, axis=-1):
    return torch.softmax(x, dim=axis)

def _gelu(x):
    return torch.nn.functional.gelu(x, approximate='tanh')

def _silu(x):
    return torch.nn.functional.silu(x)

def _sigmoid(x):
    return torch.sigmoid(x)

def _layernorm(x, axis=-1, eps=1e-05):
    m = x.mean(dim=axis, keepdim=True)
    v = x.var(dim=axis, keepdim=True, unbiased=False)
    return (x - m) / torch.sqrt(v + eps)

def _scatter_add(tgt, idx, src, axis=0):
    return tgt.clone().index_add_(axis, idx, src)

def _segment_sum(src, seg, n):
    out = torch.zeros((n,) + src.shape[1:], dtype=src.dtype)
    return out.index_add_(0, seg, src)

def _pad(x, pw):
    flat = tuple((v for pair in reversed(pw) for v in pair))
    return torch.nn.functional.pad(x, flat)
kernel_name = 'standalone__autoregressive_conv_wavenet__speech'

def setup_inputs():

    _w = lambda a: (lambda t: t.to(torch.bfloat16) if t.is_floating_point() else t)(torch.as_tensor(a))
    B, T, D, K = (2, 1000, 144, 31)
    return {'x': _w((lambda *s: torch.randn(*s, dtype=torch.bfloat16) * 0.1)(B, T, D)), 'w': _w((lambda *s: torch.randn(*s, dtype=torch.bfloat16) * 0.1)(K, D, 2 * D)), 'w_o': _w((lambda *s: torch.randn(*s, dtype=torch.bfloat16) * 0.05)(D, D))}

def reference(x, w, w_o):
    B, T, D = x.shape
    K = w.shape[0]
    dil = 2
    pad = (K - 1) * dil
    xp = _pad(x, ((0, 0), (pad, 0), (0, 0)))
    h = torch.einsum('btc,cd->btd', xp[:, pad:pad + T, :], w[0])
    for i in range(1, K):
        off = pad - i * dil
        h = h + torch.einsum('btc,cd->btd', xp[:, off:off + T, :], w[i])
    f = h[..., :D].clone()
    g = h[..., D:].clone()
    z = torch.tanh(f) * _sigmoid(g)
    return z @ w_o
```

```cpp
#include <hip/hip_runtime.h>
#include <hip/hip_bf16.h>
__global__ void k1(const __hip_bfloat16* x, const __hip_bfloat16* w, const __hip_bfloat16* wo, __hip_bfloat16* out) {
    __shared__ float xs[68][144];
    __shared__ float hs[8][288];
    __shared__ float zs[8][144];
    int row0 = blockIdx.x * 8; int b = row0 / 1000; int t0 = row0 % 1000;
    int n = threadIdx.x;
    for (int i = n; i < 68 * 144; i += 288) { int r = i / 144, c = i % 144; int t = t0 - 60 + r; xs[r][c] = t >= 0 ? __bfloat162float(x[(b * 1000 + t) * 144 + c]) : 0.f; }
    __syncthreads();
    float acc[8];
    for (int r = 0; r < 8; r++) acc[r] = 0.f;
    for (int i = 0; i < 31; i++)
        for (int c = 0; c < 144; c++) {
            float wv = __bfloat162float(w[(i * 144 + c) * 288 + n]);
            #pragma unroll
            for (int r = 0; r < 8; r++) acc[r] += xs[60 + r - 2 * i][c] * wv;
        }
    for (int r = 0; r < 8; r++) hs[r][n] = acc[r];
    __syncthreads();
    if (n < 144) for (int r = 0; r < 8; r++) { float f = hs[r][n], g = hs[r][n + 144]; zs[r][n] = __bfloat162float(__float2bfloat16(tanhf(f) / (1.f + expf(-g)))); }
    __syncthreads();
    if (n < 144) for (int r = 0; r < 8; r++) { float a = 0; for (int c = 0; c < 144; c++) a += zs[r][c] * __bfloat162float(wo[c * 144 + n]); out[(row0 + r) * 144 + n] = __float2bfloat16(a); }
}
extern "C" void kernel_launch(void* const* d_in, const int* in_sizes, int n_in, void* d_out, int out_size, void* d_ws, size_t ws_size, hipStream_t stream) {
    k1<<<250, 288, 0, stream>>>((const __hip_bfloat16*)d_in[0], (const __hip_bfloat16*)d_in[1], (const __hip_bfloat16*)d_in[2], (__hip_bfloat16*)d_out);
}
```

```cpp
#include <hip/hip_runtime.h>
#include <hip/hip_bf16.h>
#include <stdint.h>

typedef __attribute__((ext_vector_type(8))) __bf16 bf16x8;
typedef __attribute__((ext_vector_type(4))) __bf16 bf16x4;
typedef __attribute__((ext_vector_type(2))) __bf16 bf16x2;
typedef __attribute__((ext_vector_type(4))) short s16x4;
typedef __attribute__((ext_vector_type(4))) float f32x4;
typedef __attribute__((ext_vector_type(2))) float f32x2;
typedef __attribute__((ext_vector_type(4))) unsigned u32x4;
typedef __attribute__((ext_vector_type(2))) unsigned u32x2;
typedef __attribute__((ext_vector_type(4))) int i32x4;
#define LAS __attribute__((address_space(3)))
#define GAS __attribute__((address_space(1)))

constexpr int X_BYTES = 176 * 288;
constexpr int RING = X_BYTES;
constexpr int NSLOT = 27;
constexpr int ZOFF = RING + NSLOT * 4096;
constexpr int ZSTR = 304;
constexpr int LDS_BYTES = ZOFF + 8 * ZSTR;
constexpr int WO_OFF = RING + 8 * 4096;
constexpr int INBOX = 18432;
constexpr int FLAG_OFF = 14 * 18 * INBOX;
constexpr int NONCE_OFF = FLAG_OFF + 14 * 18 * 256;
constexpr int WS_BYTES = NONCE_OFF + 14 * 256;
constexpr int TAPB = 72 * 1152;
constexpr int W_BYTES = 31 * TAPB;
constexpr unsigned SPIN_CAP = 1u << 18;

__host__ __device__ constexpr int GS(int k) { return k == 0 ? 0 : k == 1 ? 1 : k == 2 ? 2 : k == 3 ? 4 : k == 4 ? 6 : k == 5 ? 9 : k == 6 ? 12 : k == 7 ? 16 : k == 8 ? 20 : k == 9 ? 24 : k == 10 ? 28 : k == 11 ? 32 : 35; }
__host__ __device__ constexpr int GI(int k) { return k < 0 ? 0 : k == 0 ? 6 : k == 1 ? 8 : k == 2 ? 11 : k == 3 ? 14 : k == 4 ? 18 : k == 5 ? 22 : k == 6 ? 27 : k == 7 ? 31 : 35; }
constexpr int NG = 12;
__host__ __device__ constexpr bool isGS(int s) { for (int k = 0; k < NG; k++) if (GS(k) == s) return true; return false; }

__device__ __forceinline__ i32x4 make_srd(const void* p, unsigned bytes) {
    unsigned long long a = (unsigned long long)p;
    i32x4 r; r[0] = (int)(unsigned)a; r[1] = (int)(unsigned)(a >> 32); r[2] = (int)bytes; r[3] = 0x00020000;
    return r;
}
__device__ __forceinline__ void dma16(i32x4 srd, unsigned voff, unsigned soff, unsigned lds) {
    asm volatile("s_mov_b32 m0, %0\n\ts_nop 0\n\tbuffer_load_dwordx4 %1, %2, %3 offen lds" :: "s"(lds), "v"(voff), "s"(srd), "s"(soff) : "memory");
}
template <int N> __device__ __forceinline__ void vmwait() { asm volatile("s_waitcnt vmcnt(%0)" :: "n"(N) : "memory"); }
__device__ __forceinline__ void bar() { asm volatile("s_barrier" ::: "memory"); }
__device__ __forceinline__ u32x4 lds128(unsigned a) { return *(LAS u32x4*)a; }
__device__ __forceinline__ s16x4 ldstr(unsigned a) { return __builtin_amdgcn_ds_read_tr16_b64_v4i16((LAS s16x4*)a); }
__device__ __forceinline__ unsigned pk2(float a, float b) { f32x2 v = {a, b}; return __builtin_bit_cast(unsigned, __builtin_convertvector(v, bf16x2)); }

template <int K> struct LoaderStage {
    static __device__ __forceinline__ void run(i32x4 sw, i32x4 swo, unsigned vw, unsigned vt, unsigned vo, unsigned sA, unsigned sB, unsigned ldsw) {
        #pragma unroll
        for (int s = GI(K - 1); s < GI(K); s++) {
            unsigned dst = ldsw + (s % NSLOT) * 4096;
            if (s < 16) dma16(sw, vw, sA + s * TAPB, dst);
            else if (s < 31) dma16(sw, vw, sB + (s - 16) * TAPB, dst);
            else dma16(sw, vt + (s - 31) * 4 * TAPB, 0, dst);
        }
        if (K == 9) {
            #pragma unroll
            for (int i = 0; i < 11; i++) dma16(swo, vo + i * 4096, 0, ldsw + 8 * 4096 + i * 4096);
        }
        vmwait<GI(K) - GS(K + 1) + (K >= 9 ? 11 : 0)>();
        bar();
        LoaderStage<K + 1>::run(sw, swo, vw, vt, vo, sA, sB, ldsw);
    }
};
template <> struct LoaderStage<NG> {
    static __device__ __forceinline__ void run(i32x4, i32x4, unsigned, unsigned, unsigned, unsigned, unsigned, unsigned) {}
};

__device__ __forceinline__ bf16x8 xfrag(unsigned a0, unsigned a1, int off, unsigned sel) {
    u32x4 h0 = lds128(a0 + off), h1 = lds128(a1 + off);
    u32x4 r;
    r[0] = __builtin_amdgcn_perm(h0[1], h0[0], sel); r[1] = __builtin_amdgcn_perm(h0[3], h0[2], sel);
    r[2] = __builtin_amdgcn_perm(h1[1], h1[0], sel); r[3] = __builtin_amdgcn_perm(h1[3], h1[2], sel);
    return __builtin_bit_cast(bf16x8, r);
}
__device__ __forceinline__ bf16x8 wfrag(unsigned a0, unsigned a1, int off) {
    s16x4 lo = ldstr(a0 + off), hi = ldstr(a1 + off);
    return __builtin_bit_cast(bf16x8, __builtin_shufflevector(lo, hi, 0, 1, 2, 3, 4, 5, 6, 7));
}

__global__ __launch_bounds__(576) void wavenet_kernel(const char* __restrict__ x, const char* __restrict__ w, const char* __restrict__ wo,
                                                      char* __restrict__ out, char* __restrict__ ws) {
    extern __shared__ __attribute__((aligned(16))) char smem[];
    const int lane = threadIdx.x & 63;
    const int wave = __builtin_amdgcn_readfirstlane(threadIdx.x >> 6);
    const int gb = blockIdx.x;
    const int lin = (gb & 7) * 32 + (gb >> 3);
    if (lin >= 252) return;
    const int ci = lin / 14, m = lin - ci * 14;
    const int kh = ci >= 9 ? 1 : 0, j = ci - 9 * kh;
    const int bb = m >= 7 ? 1 : 0, t0 = (m - 7 * bb) * 144;
    const unsigned lbase = (unsigned)(size_t)(LAS char*)smem;
    const int r = lane & 15, g = lane >> 4;
    const i32x4 sws = make_srd(ws, WS_BYTES);
    unsigned E = 0;

    if (wave < 4) {
        const int par = j < 4 ? 0 : (j > 4 ? 1 : (wave >> 1));
        const unsigned sel = par ? 0x07060302u : 0x05040100u;
        const int R = 18 * (r >> 1) + (r & 1);
        const unsigned hs = 16 * (g & 1);
        const unsigned aA = lbase + (R + 2) * 288 + 128 * kh + 32 * g + hs;
        const unsigned aB = lbase + (R + 4 - 2 * kh) * 288 + 128 * (1 - kh) + 32 * g + hs;
        const unsigned aT = lbase + (R + 8 - 2 * g - 2 * kh) * 288 + 256 + hs;
        const int q = (lane >> 2) & 3, pp = lane & 3;
        const int fk = ((q >> 1) & 1) | ((g & 1) << 1);
        const unsigned aW = lbase + RING + (8 * g + q) * 128 + (((2 * wave + (pp >> 1)) ^ (fk << 1)) * 16) + 8 * (pp & 1) + 512 * (g & 1);
        const unsigned aW1 = aW ^ 512;
        f32x4 acc[9];
        #pragma unroll
        for (int p = 0; p < 9; p++) acc[p] = (f32x4){0.f, 0.f, 0.f, 0.f};
        bf16x8 FA[24], FB[23], FT[21], WF[35];
        const unsigned aWb = aW - 512 * (g & 1), w0 = aWb + 512 * (g & 1), w1 = aWb + 512 * (1 - (g & 1));
        (void)aW1;
        bar();
        WF[0] = wfrag(w0, w1, 0);
        #pragma unroll
        for (int a = 23; a >= 15; a--) FA[a] = xfrag(aA, aA ^ 16, a * 576, sel);
        #pragma unroll
        for (int s = 0; s < 35; s++) {
            #pragma unroll
            for (int p = 8; p >= 0; p--) {
                bf16x8 xf = s < 16 ? FA[p - s + 15] : (s < 31 ? FB[p - (s - 16) + 14] : FT[p - 4 * (s - 31) + 12]);
                acc[p] = __builtin_amdgcn_mfma_f32_16x16x32_bf16(WF[s], xf, acc[p], 0, 0, 0);
                if (p == 8) {
                    __builtin_amdgcn_sched_barrier(0);
                    if (s + 1 < 35) {
                        if (isGS(s + 1)) bar();
                        WF[s + 1] = wfrag(w0, w1, ((s + 1) % NSLOT) * 4096);
                    }
                    if (s < 15) FA[14 - s] = xfrag(aA, aA ^ 16, (14 - s) * 576, sel);
                    if (s >= 7 && s < 16) FB[22 - (s - 7)] = xfrag(aB, aB ^ 16, (22 - (s - 7)) * 576, sel);
                    if (s >= 16 && s < 30) FB[13 - (s - 16)] = xfrag(aB, aB ^ 16, (13 - (s - 16)) * 576, sel);
                    if (s >= 22 && s < 31) FT[20 - (s - 22)] = xfrag(aT, aT ^ 16, (20 - (s - 22)) * 576, sel);
                    if (s >= 31 && s < 34) {
                        #pragma unroll
                        for (int a = 0; a < 4; a++) FT[11 - 4 * (s - 31) - a] = xfrag(aT, aT ^ 16, (11 - 4 * (s - 31) - a) * 576, sel);
                    }
                }
            }
            __builtin_amdgcn_sched_barrier(0);
        }
        const unsigned vq = 16 * j + 4 * wave + g;
        const unsigned ob = (m * 18 + r) * INBOX + (kh * 4 * 144 + vq) * 16;
        #pragma unroll
        for (int pl = 0; pl < 4; pl++) {
            u32x4 d;
            d[0] = min(pk2(acc[2 * pl][0], acc[2 * pl][1]), 0xFFFFFFFEu); d[1] = min(pk2(acc[2 * pl][2], acc[2 * pl][3]), 0xFFFFFFFEu);
            d[2] = min(pk2(acc[2 * pl + 1][0], acc[2 * pl + 1][1]), 0xFFFFFFFEu); d[3] = min(pk2(acc[2 * pl + 1][2], acc[2 * pl + 1][3]), 0xFFFFFFFEu);
            asm volatile("buffer_store_dwordx4 %0, %1, %2, 0 offen sc1\n\ts_nop 1" :: "v"(d), "v"(ob + pl * 144 * 16), "s"(sws) : "memory");
        }
        {
            u32x2 d;
            d[0] = min(pk2(acc[8][0], acc[8][1]), 0xFFFFFFFEu); d[1] = min(pk2(acc[8][2], acc[8][3]), 0xFFFFFFFEu);
            const unsigned o8 = (m * 18 + 16 + (r >> 3)) * INBOX + ((kh * 4 + ((r & 7) >> 1)) * 144 + vq) * 16 + 8 * (r & 1);
            asm volatile("buffer_store_dwordx2 %0, %1, %2, 0 offen sc1" :: "v"(d), "v"(o8), "s"(sws) : "memory");
        }
    } else if (wave < 8) {
        const int d = wave - 4;
        const i32x4 sx = make_srd(x + bb * 288000, 288000);
        const i32x4 sw = make_srd(w, W_BYTES);
        const i32x4 swo = make_srd(wo, 144 * 288);
        const unsigned vx = (unsigned)((t0 - 32 - 30 * kh) * 288 + d * 1024 + lane * 16);
        #pragma unroll
        for (int i = 0; i < 12; i++) dma16(sx, vx + i * 4096, 0, lbase + d * 1024 + i * 4096);
        if (lane < 24) dma16(sx, vx + 49152 - d * 640, 0, lbase + 49152 + d * 384);
        const int f = ((lane >> 4) & 1) | ((d & 1) << 1);
        const unsigned pc = (((lane & 7) ^ (f << 1)) * 16) + j * 128;
        const unsigned vw = (8 * d + (lane >> 3)) * 1152 + pc;
        const unsigned vt = ((16 * kh + d) * 72 + 64 + (lane >> 3)) * 1152 + pc;
        const unsigned sA = kh ? (15 * TAPB + 32 * 1152) : 0;
        const unsigned sB = kh ? (16 * TAPB) : (32 * 1152);
        LoaderStage<0>::run(sw, swo, vw, vt, d * 1024 + lane * 16, sA, sB, lbase + RING + d * 1024);
        vmwait<0>();
    } else {
        asm volatile("buffer_load_dword %0, off, %1, %2 sc1" : "=v"(E) : "s"(sws), "s"(NONCE_OFF + m * 256) : "memory");
        const u32x4 ones = {0xFFFFFFFFu, 0xFFFFFFFFu, 0xFFFFFFFFu, 0xFFFFFFFFu};
        const unsigned ib = (m * 18 + ci) * INBOX + lane * 16;
        #pragma unroll
        for (int i = 0; i < 18; i++) asm volatile("buffer_store_dwordx4 %0, %1, %2, 0 offen sc1" :: "v"(ones), "v"(ib + i * 1024), "s"(sws) : "memory");
        bar(); bar(); bar();
        asm volatile("s_waitcnt vmcnt(0)" : "+v"(E) :: "memory");
        E += 1;
        if (lane == 0) asm volatile("buffer_store_dword %0, off, %1, %2 sc1" :: "v"(E), "s"(sws), "s"(FLAG_OFF + (m * 18 + ci) * 256) : "memory");
        bar(); bar(); bar(); bar(); bar(); bar(); bar();
        const unsigned fo = FLAG_OFF + (m * 18 + min(lane, 17)) * 256;
        unsigned fl;
        asm volatile("buffer_load_dword %0, %1, %2, 0 offen sc1" : "=v"(fl) : "v"(fo), "s"(sws) : "memory");
        bar();
        for (unsigned spin = 0; spin < SPIN_CAP; spin++) {
            asm volatile("s_waitcnt vmcnt(0)" : "+v"(fl) :: "memory");
            if (__all(fl == E)) break;
            __builtin_amdgcn_s_sleep(2);
            asm volatile("buffer_load_dword %0, %1, %2, 0 offen sc1" : "=v"(fl) : "v"(fo), "s"(sws) : "memory");
        }
        asm volatile("s_waitcnt vmcnt(0)" ::: "memory");
        bar();
    }
    bar();
    const int idx = 16 * wave + r, plane = idx / 36, cq = idx - 36 * plane;
    const unsigned of = (m * 18 + ci) * INBOX + (((g >> 1) * 4 + plane) * 144 + (g & 1) * 72 + cq) * 16;
    u32x4 pf, pg;
    asm volatile("buffer_load_dwordx4 %0, %2, %3, 0 offen sc1\n\tbuffer_load_dwordx4 %1, %2, %3, 0 offen offset:576 sc1" : "=&v"(pf), "=&v"(pg) : "v"(of), "s"(sws) : "memory");
    bf16x8 wof[4]; s16x4 wot;
    {
        const unsigned a = lbase + WO_OFF + (8 * g + ((lane >> 2) & 3)) * 288 + 32 * wave + 8 * (lane & 3);
        #pragma unroll
        for (int s = 0; s < 4; s++) wof[s] = wfrag(a, a + 4 * 288, s * 32 * 288);
        wot = ldstr(a + (128 - 4 * g) * 288);
    }
    u32x4 sf = {0u, 0u, 0u, 0u}, sg = {0u, 0u, 0u, 0u};
    {
        const unsigned one = (r & 1) ? 0x3F800000u : 0x00003F80u;
        #pragma unroll
        for (int k = 0; k < 4; k++) { sf[k] = ((r >> 2) == k && !(r & 2)) ? one : 0u; sg[k] = ((r >> 2) == k && (r & 2)) ? one : 0u; }
    }
    for (unsigned spin = 0; spin < SPIN_CAP; spin++) {
        asm volatile("s_waitcnt vmcnt(0)" : "+v"(pf), "+v"(pg) :: "memory");
        unsigned mx = max(max(max(pf[0], pf[1]), max(pf[2], pf[3])), max(max(pg[0], pg[1]), max(pg[2], pg[3])));
        if (__all(mx != 0xFFFFFFFFu)) break;
        __builtin_amdgcn_s_sleep(1);
        asm volatile("buffer_load_dwordx4 %0, %2, %3, 0 offen sc1\n\tbuffer_load_dwordx4 %1, %2, %3, 0 offen offset:576 sc1" : "=&v"(pf), "=&v"(pg) : "v"(of), "s"(sws) : "memory");
    }
    asm volatile("s_waitcnt vmcnt(0)" : "+v"(pf), "+v"(pg) :: "memory");
    f32x4 h = {0.f, 0.f, 0.f, 0.f};
    h = __builtin_amdgcn_mfma_f32_16x16x32_bf16(__builtin_bit_cast(bf16x8, sf), __builtin_bit_cast(bf16x8, pf), h, 0, 0, 0);
    h = __builtin_amdgcn_mfma_f32_16x16x32_bf16(__builtin_bit_cast(bf16x8, sg), __builtin_bit_cast(bf16x8, pg), h, 0, 0, 0);
    float z[2];
    #pragma unroll
    for (int e = 0; e < 2; e++) {
        float ef = __builtin_amdgcn_exp2f(h[e] * 2.885390081777927f);
        float eg = __builtin_amdgcn_exp2f(h[2 + e] * -1.4426950408889634f);
        float th = 1.0f - 2.0f * __builtin_amdgcn_rcpf(ef + 1.0f);
        z[e] = th * __builtin_amdgcn_rcpf(1.0f + eg);
    }
    *(LAS unsigned*)(lbase + ZOFF + (2 * plane + (g >> 1)) * ZSTR + (4 * cq + 2 * (g & 1)) * 2) = pk2(z[0], z[1]);
    asm volatile("s_waitcnt lgkmcnt(0)\n\ts_barrier" ::: "memory");
    f32x4 o = {0.f, 0.f, 0.f, 0.f}, o2 = o, o3 = o;
    {
        const unsigned za = lbase + ZOFF + (r & 7) * ZSTR;
        #pragma unroll
        for (int s = 0; s < 2; s++) {
            o = __builtin_amdgcn_mfma_f32_16x16x32_bf16(wof[s], __builtin_bit_cast(bf16x8, lds128(za + 16 * g + 64 * s)), o, 0, 0, 0);
            o2 = __builtin_amdgcn_mfma_f32_16x16x32_bf16(wof[s + 2], __builtin_bit_cast(bf16x8, lds128(za + 16 * g + 64 * s + 128)), o2, 0, 0, 0);
        }
        s16x4 zt = *(LAS s16x4*)(za + 256 + 8 * g);
        o3 = __builtin_amdgcn_mfma_f32_16x16x16bf16_1k(wot, zt, o3, 0, 0, 0);
        o = o + o2 + o3;
    }
    int t;
    if (ci < 16) t = t0 + 18 * (ci >> 1) + (ci & 1) + 2 * r;
    else { const int rr = 8 * (ci - 16) + r; t = t0 + 18 * (rr >> 1) + (rr & 1) + 16; }
    if (r < 8 && t < 1000) {
        u32x2 ov = {pk2(o[0], o[1]), pk2(o[2], o[3])};
        *(GAS u32x2*)(out + (size_t)(bb * 1000 + t) * 288 + 32 * wave + 8 * g) = ov;
    }
    if (wave == 8 && ci == 0 && lane == 0) asm volatile("buffer_store_dword %0, off, %1, %2 sc1" :: "v"(E), "s"(sws), "s"(NONCE_OFF + m * 256) : "memory");
}

extern "C" void kernel_launch(void* const* d_in, const int* in_sizes, int n_in,
                              void* d_out, int out_size, void* d_ws, size_t ws_size,
                              hipStream_t stream) {
    (void)in_sizes; (void)n_in; (void)out_size; (void)ws_size;
    hipFuncSetAttribute((const void*)wavenet_kernel, hipFuncAttributeMaxDynamicSharedMemorySize, LDS_BYTES);
    wavenet_kernel<<<256, 576, LDS_BYTES, stream>>>((const char*)d_in[0], (const char*)d_in[1], (const char*)d_in[2], (char*)d_out, (char*)d_ws);
}
```

```cpp
#define PAD 120000
#define DLDS 163712
#include <hip/hip_runtime.h>
#include <hip/hip_bf16.h>
#define NT 320
__global__ __launch_bounds__(NT) void k1(const __hip_bfloat16* __restrict__ x, const __hip_bfloat16* __restrict__ w, const __hip_bfloat16* __restrict__ wo, __hip_bfloat16* out, float* ws) {
    unsigned long long rt0 = __builtin_amdgcn_s_memrealtime();
    int row0 = blockIdx.x * 8; int b = row0 / 1000; int t0 = row0 % 1000;
    int n = threadIdx.x;
    float* hs = ws + blockIdx.x * 4096;
    if (n < 288 && blockIdx.x < 250) {
    float acc[8];
    for (int r = 0; r < 8; r++) acc[r] = 0.f;
    for (int i = 0; i < 31; i++)
        for (int c = 0; c < 144; c += 2) {
            float w0 = __bfloat162float(w[(i * 144 + c) * 288 + n]), w1 = __bfloat162float(w[(i * 144 + c + 1) * 288 + n]);
            #pragma unroll
            for (int r = 0; r < 8; r++) { int t = t0 + r - 2 * i; unsigned u = t >= 0 ? *(const unsigned*)(x + (b * 1000 + t) * 144 + c) : 0u;
                acc[r] += __uint_as_float(u << 16) * w0 + __uint_as_float(u & 0xffff0000u) * w1; }
        }
    for (int r = 0; r < 8; r++) hs[r * 288 + n] = acc[r];
    }
    __syncthreads();
    if (n < 144 && blockIdx.x < 250) for (int r = 0; r < 8; r++) { float f = hs[r * 288 + n], g = hs[r * 288 + n + 144]; hs[2304 + r * 144 + n] = __bfloat162float(__float2bfloat16(tanhf(f) / (1.f + expf(-g)))); }
    __syncthreads();
    if (n < 144 && blockIdx.x < 250) for (int r = 0; r < 8; r++) { float a = 0; for (int c = 0; c < 144; c++) a += hs[2304 + r * 144 + c] * __bfloat162float(wo[c * 144 + n]); out[(row0 + r) * 144 + n] = __float2bfloat16(a); }
    if (n < 64) while (__builtin_amdgcn_s_memrealtime() - rt0 < PAD) {}
}
extern "C" void kernel_launch(void* const* d_in, const int* in_sizes, int n_in, void* d_out, int out_size, void* d_ws, size_t ws_size, hipStream_t stream) {
    if (DLDS) (void)hipFuncSetAttribute((const void*)k1, hipFuncAttributeMaxDynamicSharedMemorySize, DLDS);
    k1<<<256, NT, DLDS, stream>>>((const __hip_bfloat16*)d_in[0], (const __hip_bfloat16*)d_in[1], (const __hip_bfloat16*)d_in[2], (__hip_bfloat16*)d_out, (float*)d_ws);
}
```

```cpp
#include <hip/hip_runtime.h>
#include <stdint.h>

__global__ __launch_bounds__(576) void wavenet_kernel(const char* __restrict__ x, const char* __restrict__ w, const char* __restrict__ wo,
                                                      char* __restrict__ out, char* __restrict__ ws) {
#if defined(__HIP_DEVICE_COMPILE__)
    const unsigned long long ka = (unsigned long long)__builtin_amdgcn_kernarg_segment_ptr();
    const unsigned gb = __builtin_amdgcn_workgroup_id_x();
    const unsigned tid = __builtin_amdgcn_workitem_id_x();
    asm volatile(
    "s_load_dwordx2 s[16:17], s[0:1], 0x0\n"
    "s_load_dwordx2 s[20:21], s[0:1], 0x8\n"
    "s_getpc_b64 s[40:41]\n"
    ".Lwn_begin:\n"
    "s_lshr_b32 s3, s2, 3\n"
    "s_mov_b32 s42, .Lwn_end-.Lwn_begin\n"
    "s_mov_b32 s43, 0x20000\n"
    "s_lshl_b32 s4, s3, 9\n"
    "v_cmp_gt_u32 vcc, 4, v0\n"
    "v_lshl_add_u32 v1, v0, 7, s4\n"
    "s_mov_b64 exec, vcc\n"
    "buffer_load_dword v27, v1, s[40:43], 0 offen\n"
    "s_mov_b64 exec, -1\n"
    "s_load_dwordx2 s[32:33], s[0:1], 0x20\n"
    "s_load_dwordx2 s[24:25], s[0:1], 0x10\n"
    "s_load_dwordx2 s[28:29], s[0:1], 0x18\n"
    "v_readfirstlane_b32 s13, v0\n"
    "s_and_b32 s5, s2, 1\n"
    "s_bfe_u32 s10, s2, 0x10001\n"
    "s_bfe_u32 s6, s2, 0x10002\n"
    "s_lshl_b32 s7, s5, 5\n"
    "s_add_u32 s7, s7, s3\n"
    "s_mul_i32 s8, s7, 37\n"
    "s_lshr_b32 s8, s8, 8\n"
    "s_mul_i32 s9, s8, 7\n"
    "s_sub_u32 s9, s7, s9\n"
    "s_mul_i32 s11, s9, 144\n"
    "s_mul_i32 s46, s10, 288000\n"
    "s_mul_i32 s4, s10, 7\n"
    "s_add_u32 s9, s9, s4\n"
    "s_lshr_b32 s13, s13, 6\n"
    "s_movk_i32 s47, 1152\n"
    "s_mul_i32 s4, s6, 30\n"
    "s_sub_u32 s4, s11, s4\n"
    "s_mul_i32 s4, s4, 288\n"
    "s_lshl_b32 s48, s13, 4\n"
    "s_sub_u32 s4, s4, s48\n"
    "s_sub_u32 s4, s4, 32*288\n"
    "v_and_b32 v28, 63, v0\n"
    "v_lshl_add_u32 v4, v0, 4, s4\n"
    "v_bfe_u32 v5, v28, 4, 1\n"
    "s_and_b32 s4, s13, 1\n"
    "s_lshl_b32 s4, s4, 1\n"
    "v_or_b32 v5, s4, v5\n"
    "v_lshlrev_b32 v5, 1, v5\n"
    "v_and_b32 v6, 7, v28\n"
    "v_xor_b32 v5, v5, v6\n"
    "s_lshl_b32 s4, s8, 7\n"
    "v_lshl_add_u32 v5, v5, 4, s4\n"
    "v_lshrrev_b32 v6, 3, v28\n"
    "v_mad_u32_u24 v21, v6, s47, v5\n"
    "s_sub_u32 s50, s13, 4\n"
    "s_mul_i32 s4, s50, 8*1152\n"
    "v_add_u32 v16, s4, v21\n"
    "s_mul_i32 s51, s6, 15*82944+32*1152\n"
    "s_lshl_b32 s52, s50, 10\n"
    "s_add_u32 m0, s52, 50816\n"
    "s_mov_b32 s18, 288000\n"
    "s_mov_b32 s19, 0x20000\n"
    "s_mov_b32 s22, 2571264\n"
    "s_mov_b32 s23, 0x20000\n"
    "s_cmp_eq_u32 s13, 8\n"
    "s_waitcnt lgkmcnt(0)\n"
    "s_cbranch_scc1 .Lwn_ctl\n"
    "s_cmp_ge_u32 s7, 63\n"
    "s_cbranch_scc1 .Lwn_exit\n"
    "s_add_u32 s16, s16, s46\n"
    "s_addc_u32 s17, s17, 0\n"
    "s_cmp_ge_u32 s13, 4\n"
    "s_cbranch_scc1 .Lwn_notm\n"
    "buffer_load_dwordx4 v[76:79], v4, s[16:19], 0 offen\n"
    "v_add_u32 v4, 4032, v4\n"
    "buffer_load_dwordx4 v[80:83], v4, s[16:19], 0 offen\n"
    "v_add_u32 v4, 4032, v4\n"
    "buffer_load_dwordx4 v[84:87], v4, s[16:19], 0 offen\n"
    "v_add_u32 v4, 4032, v4\n"
    "buffer_load_dwordx4 v[88:91], v4, s[16:19], 0 offen\n"
    "v_add_u32 v4, 4032, v4\n"
    "buffer_load_dwordx4 v[92:95], v4, s[16:19], 0 offen\n"
    "v_add_u32 v4, 4032, v4\n"
    "buffer_load_dwordx4 v[96:99], v4, s[16:19], 0 offen\n"
    "v_add_u32 v4, 4032, v4\n"
    "buffer_load_dwordx4 v[100:103], v4, s[16:19], 0 offen\n"
    "v_add_u32 v4, 4032, v4\n"
    "buffer_load_dwordx4 v[104:107], v4, s[16:19], 0 offen\n"
    "v_add_u32 v4, 4032, v4\n"
    "buffer_load_dwordx4 v[108:111], v4, s[16:19], 0 offen\n"
    "v_add_u32 v4, 4032, v4\n"
    "buffer_load_dwordx4 v[112:115], v4, s[16:19], 0 offen\n"
    "v_add_u32 v4, 4032, v4\n"
    "buffer_load_dwordx4 v[116:119], v4, s[16:19], 0 offen\n"
    "v_add_u32 v4, 4032, v4\n"
    "buffer_load_dwordx4 v[120:123], v4, s[16:19], 0 offen\n"
    "v_add_u32 v4, 4032, v4\n"
    "buffer_load_dwordx4 v[124:127], v4, s[16:19], 0 offen\n"
    "s_branch .Lwn_mf\n"
    ".Lwn_notm:\n"
    "s_nop 0\n"
    "buffer_load_dwordx4 v16, s[20:23], s51 offen lds\n"
    "s_add_u32 m0, m0, 0x1000\n"
    "s_add_u32 s51, s51, 82944\n"
    "buffer_load_dwordx4 v16, s[20:23], s51 offen lds\n"
    "s_branch .Lwn_ld2\n"
    ".Lwn_mf:\n"
    "v_and_b32 v32, 15, v28\n"
    "v_lshrrev_b32 v33, 4, v28\n"
    "v_and_b32 v37, 1, v33\n"
    "v_bfe_u32 v38, v28, 2, 2\n"
    "v_lshl_add_u32 v38, v33, 3, v38\n"
    "v_lshrrev_b32 v40, 1, v32\n"
    "s_mov_b32 s34, 4712960\n"
    "s_mov_b32 s35, 0x20000\n"
    "s_lshr_b32 s4, s13, 1\n"
    "s_lshl_b32 s15, s8, 1\n"
    "s_add_u32 s4, s4, s15\n"
    "s_cmp_ge_u32 s4, 9\n"
    "s_cselect_b32 s57, 144, 0\n"
    "v_and_b32 v45, 1, v32\n"
    "v_mul_u32_u24 v31, 25472, v45\n"
    "s_movk_i32 s4, 2592\n"
    "v_mad_u32_u24 v31, v40, s4, v31\n"
    "v_add_u32 v31, s57, v31\n"
    "v_lshl_add_u32 v44, v33, 4, v31\n"
    "s_lshl_b32 s4, s6, 6\n"
    "s_add_u32 s4, s4, 288\n"
    "v_add_u32 v18, s4, v44\n"
    "s_mul_i32 s4, s6, 288+64\n"
    "s_sub_u32 s4, 2*288+64, s4\n"
    "v_add_u32 v19, s4, v44\n"
    "s_mul_i32 s4, s6, 288\n"
    "s_sub_u32 s4, 4*288+128, s4\n"
    "v_mul_u32_u24 v45, 288, v33\n"
    "v_sub_u32 v20, v31, v45\n"
    "v_add_u32 v20, s4, v20\n"
    "v_subrev_u32 v44, s13, v0\n"
    "v_mul_u32_u24 v45, 3641, v44\n"
    "v_lshrrev_b32 v45, 16, v45\n"
    "v_and_b32 v46, 1, v45\n"
    "v_mul_u32_u24 v46, 25472-144, v46\n"
    "v_lshl_add_u32 v22, v44, 3, v46\n"
    "s_mov_b32 s15, 0x05040100\n"
    "s_mov_b32 s49, 0x07060302\n"
    "s_cmp_gt_u32 s8, 4\n"
    "s_cselect_b32 s15, s49, s15\n"
    "s_cselect_b32 s4, 144, 0\n"
    "v_add_u32 v22, s4, v22\n"
    "v_cmp_gt_u32 vcc, 0x90, v44\n"
    "v_bfe_u32 v44, v28, 3, 1\n"
    "v_lshl_or_b32 v44, v37, 1, v44\n"
    "v_lshlrev_b32 v44, 1, v44\n"
    "v_bfe_u32 v45, v28, 1, 1\n"
    "s_lshl_b32 s4, s13, 1\n"
    "v_or_b32 v45, s4, v45\n"
    "v_xor_b32 v44, v44, v45\n"
    "v_lshlrev_b32 v44, 4, v44\n"
    "v_lshl_add_u32 v44, v38, 7, v44\n"
    "v_and_b32 v45, 1, v28\n"
    "v_lshl_add_u32 v44, v45, 3, v44\n"
    "v_add_u32 v16, 50816, v44\n"
    "v_add_u32 v17, 0x10000, v16\n"
    "s_mul_i32 s4, s9, 18*18432\n"
    "s_mul_i32 s57, s6, 9216\n"
    "s_add_u32 s4, s4, s57\n"
    "s_lshl_b32 s57, s8, 8\n"
    "s_add_u32 s4, s4, s57\n"
    "s_lshl_b32 s57, s13, 6\n"
    "s_add_u32 s4, s4, s57\n"
    "v_lshl_add_u32 v44, v33, 4, s4\n"
    "s_movk_i32 s57, 18432\n"
    "v_mad_u32_u24 v24, v32, s57, v44\n"
    "v_add_u32 v25, 4608, v24\n"
    "v_lshrrev_b32 v45, 3, v32\n"
    "v_add_u32 v45, 16, v45\n"
    "v_mad_u32_u24 v26, v45, s57, v44\n"
    "v_and_b32 v45, 3, v40\n"
    "s_movk_i32 s57, 2304\n"
    "v_mad_u32_u24 v26, v45, s57, v26\n"
    "v_and_b32 v45, 1, v32\n"
    "v_lshl_add_u32 v26, v45, 3, v26\n"
    "s_cmp_lg_u32 s8, 4\n"
    "s_cbranch_scc1 .Lwn_x1\n"
    "s_waitcnt vmcnt(12)\n"
    "v_perm_b32 v128, v77, v76, s15\n"
    "v_perm_b32 v129, v79, v78, s15\n"
    "ds_write_b64 v22, v[128:129] offset:0\n"
    "v_perm_b32 v130, v77, v76, s49\n"
    "v_perm_b32 v131, v79, v78, s49\n"
    "ds_write_b64 v22, v[130:131] offset:144\n"
    "s_waitcnt vmcnt(11)\n"
    "v_perm_b32 v132, v81, v80, s15\n"
    "v_perm_b32 v133, v83, v82, s15\n"
    "ds_write_b64 v22, v[132:133] offset:2016\n"
    "v_perm_b32 v134, v81, v80, s49\n"
    "v_perm_b32 v135, v83, v82, s49\n"
    "ds_write_b64 v22, v[134:135] offset:2160\n"
    "s_waitcnt vmcnt(10)\n"
    "v_perm_b32 v136, v85, v84, s15\n"
    "v_perm_b32 v137, v87, v86, s15\n"
    "ds_write_b64 v22, v[136:137] offset:4032\n"
    "v_perm_b32 v138, v85, v84, s49\n"
    "v_perm_b32 v139, v87, v86, s49\n"
    "ds_write_b64 v22, v[138:139] offset:4176\n"
    "s_waitcnt vmcnt(9)\n"
    "v_perm_b32 v140, v89, v88, s15\n"
    "v_perm_b32 v141, v91, v90, s15\n"
    "ds_write_b64 v22, v[140:141] offset:6048\n"
    "v_perm_b32 v142, v89, v88, s49\n"
    "v_perm_b32 v143, v91, v90, s49\n"
    "ds_write_b64 v22, v[142:143] offset:6192\n"
    "s_waitcnt vmcnt(8)\n"
    "v_perm_b32 v128, v93, v92, s15\n"
    "v_perm_b32 v129, v95, v94, s15\n"
    "ds_write_b64 v22, v[128:129] offset:8064\n"
    "v_perm_b32 v130, v93, v92, s49\n"
    "v_perm_b32 v131, v95, v94, s49\n"
    "ds_write_b64 v22, v[130:131] offset:8208\n"
    "s_waitcnt vmcnt(7)\n"
    "v_perm_b32 v132, v97, v96, s15\n"
    "v_perm_b32 v133, v99, v98, s15\n"
    "ds_write_b64 v22, v[132:133] offset:10080\n"
    "v_perm_b32 v134, v97, v96, s49\n"
    "v_perm_b32 v135, v99, v98, s49\n"
    "ds_write_b64 v22, v[134:135] offset:10224\n"
    "s_waitcnt vmcnt(6)\n"
    "v_perm_b32 v136, v101, v100, s15\n"
    "v_perm_b32 v137, v103, v102, s15\n"
    "ds_write_b64 v22, v[136:137] offset:12096\n"
    "v_perm_b32 v138, v101, v100, s49\n"
    "v_perm_b32 v139, v103, v102, s49\n"
    "ds_write_b64 v22, v[138:139] offset:12240\n"
    "s_waitcnt vmcnt(5)\n"
    "v_perm_b32 v140, v105, v104, s15\n"
    "v_perm_b32 v141, v107, v106, s15\n"
    "ds_write_b64 v22, v[140:141] offset:14112\n"
    "v_perm_b32 v142, v105, v104, s49\n"
    "v_perm_b32 v143, v107, v106, s49\n"
    "ds_write_b64 v22, v[142:143] offset:14256\n"
    "s_waitcnt vmcnt(4)\n"
    "v_perm_b32 v128, v109, v108, s15\n"
    "v_perm_b32 v129, v111, v110, s15\n"
    "ds_write_b64 v22, v[128:129] offset:16128\n"
    "v_perm_b32 v130, v109, v108, s49\n"
    "v_perm_b32 v131, v111, v110, s49\n"
    "ds_write_b64 v22, v[130:131] offset:16272\n"
    "s_waitcnt vmcnt(3)\n"
    "v_perm_b32 v132, v113, v112, s15\n"
    "v_perm_b32 v133, v115, v114, s15\n"
    "ds_write_b64 v22, v[132:133] offset:18144\n"
    "v_perm_b32 v134, v113, v112, s49\n"
    "v_perm_b32 v135, v115, v114, s49\n"
    "ds_write_b64 v22, v[134:135] offset:18288\n"
    "s_waitcnt vmcnt(2)\n"
    "v_perm_b32 v136, v117, v116, s15\n"
    "v_perm_b32 v137, v119, v118, s15\n"
    "ds_write_b64 v22, v[136:137] offset:20160\n"
    "v_perm_b32 v138, v117, v116, s49\n"
    "v_perm_b32 v139, v119, v118, s49\n"
    "ds_write_b64 v22, v[138:139] offset:20304\n"
    "s_waitcnt vmcnt(1)\n"
    "v_perm_b32 v140, v121, v120, s15\n"
    "v_perm_b32 v141, v123, v122, s15\n"
    "ds_write_b64 v22, v[140:141] offset:22176\n"
    "v_perm_b32 v142, v121, v120, s49\n"
    "v_perm_b32 v143, v123, v122, s49\n"
    "ds_write_b64 v22, v[142:143] offset:22320\n"
    "s_waitcnt vmcnt(0)\n"
    "s_mov_b64 exec, vcc\n"
    "v_perm_b32 v128, v125, v124, s15\n"
    "v_perm_b32 v129, v127, v126, s15\n"
    "ds_write_b64 v22, v[128:129] offset:24192\n"
    "v_perm_b32 v130, v125, v124, s49\n"
    "v_perm_b32 v131, v127, v126, s49\n"
    "ds_write_b64 v22, v[130:131] offset:24336\n"
    "s_mov_b64 exec, -1\n"
    "s_branch .Lwn_x2\n"
    ".Lwn_x1:\n"
    "s_waitcnt vmcnt(12)\n"
    "v_perm_b32 v128, v77, v76, s15\n"
    "v_perm_b32 v129, v79, v78, s15\n"
    "ds_write_b64 v22, v[128:129] offset:0\n"
    "s_waitcnt vmcnt(11)\n"
    "v_perm_b32 v132, v81, v80, s15\n"
    "v_perm_b32 v133, v83, v82, s15\n"
    "ds_write_b64 v22, v[132:133] offset:2016\n"
    "s_waitcnt vmcnt(10)\n"
    "v_perm_b32 v136, v85, v84, s15\n"
    "v_perm_b32 v137, v87, v86, s15\n"
    "ds_write_b64 v22, v[136:137] offset:4032\n"
    "s_waitcnt vmcnt(9)\n"
    "v_perm_b32 v140, v89, v88, s15\n"
    "v_perm_b32 v141, v91, v90, s15\n"
    "ds_write_b64 v22, v[140:141] offset:6048\n"
    "s_waitcnt vmcnt(8)\n"
    "v_perm_b32 v128, v93, v92, s15\n"
    "v_perm_b32 v129, v95, v94, s15\n"
    "ds_write_b64 v22, v[128:129] offset:8064\n"
    "s_waitcnt vmcnt(7)\n"
    "v_perm_b32 v132, v97, v96, s15\n"
    "v_perm_b32 v133, v99, v98, s15\n"
    "ds_write_b64 v22, v[132:133] offset:10080\n"
    "s_waitcnt vmcnt(6)\n"
    "v_perm_b32 v136, v101, v100, s15\n"
    "v_perm_b32 v137, v103, v102, s15\n"
    "ds_write_b64 v22, v[136:137] offset:12096\n"
    "s_waitcnt vmcnt(5)\n"
    "v_perm_b32 v140, v105, v104, s15\n"
    "v_perm_b32 v141, v107, v106, s15\n"
    "ds_write_b64 v22, v[140:141] offset:14112\n"
    "s_waitcnt vmcnt(4)\n"
    "v_perm_b32 v128, v109, v108, s15\n"
    "v_perm_b32 v129, v111, v110, s15\n"
    "ds_write_b64 v22, v[128:129] offset:16128\n"
    "s_waitcnt vmcnt(3)\n"
    "v_perm_b32 v132, v113, v112, s15\n"
    "v_perm_b32 v133, v115, v114, s15\n"
    "ds_write_b64 v22, v[132:133] offset:18144\n"
    "s_waitcnt vmcnt(2)\n"
    "v_perm_b32 v136, v117, v116, s15\n"
    "v_perm_b32 v137, v119, v118, s15\n"
    "ds_write_b64 v22, v[136:137] offset:20160\n"
    "s_waitcnt vmcnt(1)\n"
    "v_perm_b32 v140, v121, v120, s15\n"
    "v_perm_b32 v141, v123, v122, s15\n"
    "ds_write_b64 v22, v[140:141] offset:22176\n"
    "s_waitcnt vmcnt(0)\n"
    "s_mov_b64 exec, vcc\n"
    "v_perm_b32 v128, v125, v124, s15\n"
    "v_perm_b32 v129, v127, v126, s15\n"
    "ds_write_b64 v22, v[128:129] offset:24192\n"
    "s_mov_b64 exec, -1\n"
    ".Lwn_x2:\n"
    "s_waitcnt lgkmcnt(0)\n"
    "s_barrier\n"
    ".p2align 3\n"
    "ds_read_b64_tr_b16 v[68:69], v16 offset:0\n"
    "ds_read_b64_tr_b16 v[70:71], v16 offset:512\n"
    "ds_read_b128 v[76:79], v18 offset:6624\n"
    "ds_read_b128 v[80:83], v18 offset:6336\n"
    "ds_read_b128 v[84:87], v18 offset:6048\n"
    "ds_read_b128 v[88:91], v18 offset:5760\n"
    "ds_read_b128 v[92:95], v18 offset:5472\n"
    "ds_read_b128 v[96:99], v18 offset:5184\n"
    "ds_read_b128 v[100:103], v18 offset:4896\n"
    "ds_read_b128 v[104:107], v18 offset:4608\n"
    "ds_read_b128 v[108:111], v18 offset:4320\n"
    "s_waitcnt lgkmcnt(8)\n"
    "s_nop 0\n"
    "v_mfma_f32_16x16x32_bf16 v[64:67], v[68:71], v[76:79], 0\n"
    "ds_read_b64_tr_b16 v[72:73], v16 offset:4096\n"
    "ds_read_b64_tr_b16 v[74:75], v16 offset:4608\n"
    "s_waitcnt lgkmcnt(9)\n"
    "s_nop 0\n"
    "v_mfma_f32_16x16x32_bf16 v[60:63], v[68:71], v[80:83], 0\n"
    "ds_read_b128 v[112:115], v18 offset:4032\n"
    "s_waitcnt lgkmcnt(9)\n"
    "s_nop 0\n"
    "v_mfma_f32_16x16x32_bf16 v[56:59], v[68:71], v[84:87], 0\n"
    "s_waitcnt lgkmcnt(8)\n"
    "s_nop 0\n"
    "v_mfma_f32_16x16x32_bf16 v[52:55], v[68:71], v[88:91], 0\n"
    "s_waitcnt lgkmcnt(7)\n"
    "s_nop 0\n"
    "v_mfma_f32_16x16x32_bf16 v[48:51], v[68:71], v[92:95], 0\n"
    "s_waitcnt lgkmcnt(6)\n"
    "s_nop 0\n"
    "v_mfma_f32_16x16x32_bf16 v[44:47], v[68:71], v[96:99], 0\n"
    "s_waitcnt lgkmcnt(5)\n"
    "s_nop 0\n"
    "v_mfma_f32_16x16x32_bf16 v[40:43], v[68:71], v[100:103], 0\n"
    "s_waitcnt lgkmcnt(4)\n"
    "s_nop 0\n"
    "v_mfma_f32_16x16x32_bf16 v[36:39], v[68:71], v[104:107], 0\n"
    "s_waitcnt lgkmcnt(3)\n"
    "s_nop 0\n"
    "v_mfma_f32_16x16x32_bf16 v[32:35], v[68:71], v[108:111], 0\n"
    "s_waitcnt lgkmcnt(0)\n"
    "s_barrier\n"
    "v_mfma_f32_16x16x32_bf16 v[64:67], v[72:75], v[80:83], v[64:67]\n"
    "ds_read_b64_tr_b16 v[68:69], v16 offset:8192\n"
    "ds_read_b64_tr_b16 v[70:71], v16 offset:8704\n"
    "v_mfma_f32_16x16x32_bf16 v[60:63], v[72:75], v[84:87], v[60:63]\n"
    "ds_read_b128 v[116:119], v18 offset:3744\n"
    "v_mfma_f32_16x16x32_bf16 v[56:59], v[72:75], v[88:91], v[56:59]\n"
    "v_mfma_f32_16x16x32_bf16 v[52:55], v[72:75], v[92:95], v[52:55]\n"
    "v_mfma_f32_16x16x32_bf16 v[48:51], v[72:75], v[96:99], v[48:51]\n"
    "v_mfma_f32_16x16x32_bf16 v[44:47], v[72:75], v[100:103], v[44:47]\n"
    "v_mfma_f32_16x16x32_bf16 v[40:43], v[72:75], v[104:107], v[40:43]\n"
    "v_mfma_f32_16x16x32_bf16 v[36:39], v[72:75], v[108:111], v[36:39]\n"
    "v_mfma_f32_16x16x32_bf16 v[32:35], v[72:75], v[112:115], v[32:35]\n"
    "s_waitcnt lgkmcnt(0)\n"
    "s_nop 0\n"
    "v_mfma_f32_16x16x32_bf16 v[64:67], v[68:71], v[84:87], v[64:67]\n"
    "ds_read_b64_tr_b16 v[72:73], v16 offset:12288\n"
    "ds_read_b64_tr_b16 v[74:75], v16 offset:12800\n"
    "v_mfma_f32_16x16x32_bf16 v[60:63], v[68:71], v[88:91], v[60:63]\n"
    "ds_read_b128 v[120:123], v18 offset:3456\n"
    "v_mfma_f32_16x16x32_bf16 v[56:59], v[68:71], v[92:95], v[56:59]\n"
    "v_mfma_f32_16x16x32_bf16 v[52:55], v[68:71], v[96:99], v[52:55]\n"
    "v_mfma_f32_16x16x32_bf16 v[48:51], v[68:71], v[100:103], v[48:51]\n"
    "v_mfma_f32_16x16x32_bf16 v[44:47], v[68:71], v[104:107], v[44:47]\n"
    "v_mfma_f32_16x16x32_bf16 v[40:43], v[68:71], v[108:111], v[40:43]\n"
    "v_mfma_f32_16x16x32_bf16 v[36:39], v[68:71], v[112:115], v[36:39]\n"
    "v_mfma_f32_16x16x32_bf16 v[32:35], v[68:71], v[116:119], v[32:35]\n"
    "s_waitcnt lgkmcnt(0)\n"
    "s_barrier\n"
    "v_mfma_f32_16x16x32_bf16 v[64:67], v[72:75], v[88:91], v[64:67]\n"
    "ds_read_b64_tr_b16 v[68:69], v16 offset:16384\n"
    "ds_read_b64_tr_b16 v[70:71], v16 offset:16896\n"
    "v_mfma_f32_16x16x32_bf16 v[60:63], v[72:75], v[92:95], v[60:63]\n"
    "ds_read_b128 v[124:127], v18 offset:3168\n"
    "v_mfma_f32_16x16x32_bf16 v[56:59], v[72:75], v[96:99], v[56:59]\n"
    "v_mfma_f32_16x16x32_bf16 v[52:55], v[72:75], v[100:103], v[52:55]\n"
    "v_mfma_f32_16x16x32_bf16 v[48:51], v[72:75], v[104:107], v[48:51]\n"
    "v_mfma_f32_16x16x32_bf16 v[44:47], v[72:75], v[108:111], v[44:47]\n"
    "v_mfma_f32_16x16x32_bf16 v[40:43], v[72:75], v[112:115], v[40:43]\n"
    "v_mfma_f32_16x16x32_bf16 v[36:39], v[72:75], v[116:119], v[36:39]\n"
    "v_mfma_f32_16x16x32_bf16 v[32:35], v[72:75], v[120:123], v[32:35]\n"
    "s_waitcnt lgkmcnt(0)\n"
    "s_nop 0\n"
    "v_mfma_f32_16x16x32_bf16 v[64:67], v[68:71], v[92:95], v[64:67]\n"
    "ds_read_b64_tr_b16 v[72:73], v16 offset:20480\n"
    "ds_read_b64_tr_b16 v[74:75], v16 offset:20992\n"
    "v_mfma_f32_16x16x32_bf16 v[60:63], v[68:71], v[96:99], v[60:63]\n"
    "ds_read_b128 v[128:131], v18 offset:2880\n"
    "v_mfma_f32_16x16x32_bf16 v[56:59], v[68:71], v[100:103], v[56:59]\n"
    "v_mfma_f32_16x16x32_bf16 v[52:55], v[68:71], v[104:107], v[52:55]\n"
    "v_mfma_f32_16x16x32_bf16 v[48:51], v[68:71], v[108:111], v[48:51]\n"
    "v_mfma_f32_16x16x32_bf16 v[44:47], v[68:71], v[112:115], v[44:47]\n"
    "v_mfma_f32_16x16x32_bf16 v[40:43], v[68:71], v[116:119], v[40:43]\n"
    "v_mfma_f32_16x16x32_bf16 v[36:39], v[68:71], v[120:123], v[36:39]\n"
    "v_mfma_f32_16x16x32_bf16 v[32:35], v[68:71], v[124:127], v[32:35]\n"
    "s_waitcnt lgkmcnt(0)\n"
    "s_nop 0\n"
    "v_mfma_f32_16x16x32_bf16 v[64:67], v[72:75], v[96:99], v[64:67]\n"
    "ds_read_b64_tr_b16 v[68:69], v16 offset:24576\n"
    "ds_read_b64_tr_b16 v[70:71], v16 offset:25088\n"
    "v_mfma_f32_16x16x32_bf16 v[60:63], v[72:75], v[100:103], v[60:63]\n"
    "ds_read_b128 v[132:135], v18 offset:2592\n"
    "v_mfma_f32_16x16x32_bf16 v[56:59], v[72:75], v[104:107], v[56:59]\n"
    "v_mfma_f32_16x16x32_bf16 v[52:55], v[72:75], v[108:111], v[52:55]\n"
    "v_mfma_f32_16x16x32_bf16 v[48:51], v[72:75], v[112:115], v[48:51]\n"
    "v_mfma_f32_16x16x32_bf16 v[44:47], v[72:75], v[116:119], v[44:47]\n"
    "v_mfma_f32_16x16x32_bf16 v[40:43], v[72:75], v[120:123], v[40:43]\n"
    "v_mfma_f32_16x16x32_bf16 v[36:39], v[72:75], v[124:127], v[36:39]\n"
    "v_mfma_f32_16x16x32_bf16 v[32:35], v[72:75], v[128:131], v[32:35]\n"
    "s_waitcnt lgkmcnt(0)\n"
    "s_barrier\n"
    "v_mfma_f32_16x16x32_bf16 v[64:67], v[68:71], v[100:103], v[64:67]\n"
    "ds_read_b64_tr_b16 v[72:73], v16 offset:28672\n"
    "ds_read_b64_tr_b16 v[74:75], v16 offset:29184\n"
    "v_mfma_f32_16x16x32_bf16 v[60:63], v[68:71], v[104:107], v[60:63]\n"
    "ds_read_b128 v[136:139], v18 offset:2304\n"
    "v_mfma_f32_16x16x32_bf16 v[56:59], v[68:71], v[108:111], v[56:59]\n"
    "v_mfma_f32_16x16x32_bf16 v[52:55], v[68:71], v[112:115], v[52:55]\n"
    "v_mfma_f32_16x16x32_bf16 v[48:51], v[68:71], v[116:119], v[48:51]\n"
    "v_mfma_f32_16x16x32_bf16 v[44:47], v[68:71], v[120:123], v[44:47]\n"
    "v_mfma_f32_16x16x32_bf16 v[40:43], v[68:71], v[124:127], v[40:43]\n"
    "v_mfma_f32_16x16x32_bf16 v[36:39], v[68:71], v[128:131], v[36:39]\n"
    "v_mfma_f32_16x16x32_bf16 v[32:35], v[68:71], v[132:135], v[32:35]\n"
    "s_waitcnt lgkmcnt(0)\n"
    "s_nop 0\n"
    "v_mfma_f32_16x16x32_bf16 v[64:67], v[72:75], v[104:107], v[64:67]\n"
    "ds_read_b64_tr_b16 v[68:69], v16 offset:32768\n"
    "ds_read_b64_tr_b16 v[70:71], v16 offset:33280\n"
    "v_mfma_f32_16x16x32_bf16 v[60:63], v[72:75], v[108:111], v[60:63]\n"
    "ds_read_b128 v[140:143], v18 offset:2016\n"
    "v_mfma_f32_16x16x32_bf16 v[56:59], v[72:75], v[112:115], v[56:59]\n"
    "ds_read_b128 v[144:147], v19 offset:6336\n"
    "v_mfma_f32_16x16x32_bf16 v[52:55], v[72:75], v[116:119], v[52:55]\n"
    "v_mfma_f32_16x16x32_bf16 v[48:51], v[72:75], v[120:123], v[48:51]\n"
    "v_mfma_f32_16x16x32_bf16 v[44:47], v[72:75], v[124:127], v[44:47]\n"
    "v_mfma_f32_16x16x32_bf16 v[40:43], v[72:75], v[128:131], v[40:43]\n"
    "v_mfma_f32_16x16x32_bf16 v[36:39], v[72:75], v[132:135], v[36:39]\n"
    "v_mfma_f32_16x16x32_bf16 v[32:35], v[72:75], v[136:139], v[32:35]\n"
    "s_waitcnt lgkmcnt(0)\n"
    "s_nop 0\n"
    "v_mfma_f32_16x16x32_bf16 v[64:67], v[68:71], v[108:111], v[64:67]\n"
    "ds_read_b64_tr_b16 v[72:73], v16 offset:36864\n"
    "ds_read_b64_tr_b16 v[74:75], v16 offset:37376\n"
    "v_mfma_f32_16x16x32_bf16 v[60:63], v[68:71], v[112:115], v[60:63]\n"
    "ds_read_b128 v[148:151], v18 offset:1728\n"
    "v_mfma_f32_16x16x32_bf16 v[56:59], v[68:71], v[116:119], v[56:59]\n"
    "ds_read_b128 v[152:155], v19 offset:6048\n"
    "v_mfma_f32_16x16x32_bf16 v[52:55], v[68:71], v[120:123], v[52:55]\n"
    "v_mfma_f32_16x16x32_bf16 v[48:51], v[68:71], v[124:127], v[48:51]\n"
    "v_mfma_f32_16x16x32_bf16 v[44:47], v[68:71], v[128:131], v[44:47]\n"
    "v_mfma_f32_16x16x32_bf16 v[40:43], v[68:71], v[132:135], v[40:43]\n"
    "v_mfma_f32_16x16x32_bf16 v[36:39], v[68:71], v[136:139], v[36:39]\n"
    "v_mfma_f32_16x16x32_bf16 v[32:35], v[68:71], v[140:143], v[32:35]\n"
    "s_waitcnt lgkmcnt(0)\n"
    "s_nop 0\n"
    "v_mfma_f32_16x16x32_bf16 v[64:67], v[72:75], v[112:115], v[64:67]\n"
    "ds_read_b64_tr_b16 v[68:69], v16 offset:40960\n"
    "ds_read_b64_tr_b16 v[70:71], v16 offset:41472\n"
    "v_mfma_f32_16x16x32_bf16 v[60:63], v[72:75], v[116:119], v[60:63]\n"
    "ds_read_b128 v[156:159], v18 offset:1440\n"
    "v_mfma_f32_16x16x32_bf16 v[56:59], v[72:75], v[120:123], v[56:59]\n"
    "ds_read_b128 v[160:163], v19 offset:5760\n"
    "v_mfma_f32_16x16x32_bf16 v[52:55], v[72:75], v[124:127], v[52:55]\n"
    "v_mfma_f32_16x16x32_bf16 v[48:51], v[72:75], v[128:131], v[48:51]\n"
    "v_mfma_f32_16x16x32_bf16 v[44:47], v[72:75], v[132:135], v[44:47]\n"
    "v_mfma_f32_16x16x32_bf16 v[40:43], v[72:75], v[136:139], v[40:43]\n"
    "v_mfma_f32_16x16x32_bf16 v[36:39], v[72:75], v[140:143], v[36:39]\n"
    "v_mfma_f32_16x16x32_bf16 v[32:35], v[72:75], v[148:151], v[32:35]\n"
    "s_waitcnt lgkmcnt(0)\n"
    "s_nop 0\n"
    "v_mfma_f32_16x16x32_bf16 v[64:67], v[68:71], v[116:119], v[64:67]\n"
    "ds_read_b64_tr_b16 v[72:73], v16 offset:45056\n"
    "ds_read_b64_tr_b16 v[74:75], v16 offset:45568\n"
    "v_mfma_f32_16x16x32_bf16 v[60:63], v[68:71], v[120:123], v[60:63]\n"
    "ds_read_b128 v[164:167], v18 offset:1152\n"
    "v_mfma_f32_16x16x32_bf16 v[56:59], v[68:71], v[124:127], v[56:59]\n"
    "ds_read_b128 v[76:79], v19 offset:5472\n"
    "v_mfma_f32_16x16x32_bf16 v[52:55], v[68:71], v[128:131], v[52:55]\n"
    "v_mfma_f32_16x16x32_bf16 v[48:51], v[68:71], v[132:135], v[48:51]\n"
    "v_mfma_f32_16x16x32_bf16 v[44:47], v[68:71], v[136:139], v[44:47]\n"
    "v_mfma_f32_16x16x32_bf16 v[40:43], v[68:71], v[140:143], v[40:43]\n"
    "v_mfma_f32_16x16x32_bf16 v[36:39], v[68:71], v[148:151], v[36:39]\n"
    "v_mfma_f32_16x16x32_bf16 v[32:35], v[68:71], v[156:159], v[32:35]\n"
    "s_waitcnt lgkmcnt(0)\n"
    "s_nop 0\n"
    "v_mfma_f32_16x16x32_bf16 v[64:67], v[72:75], v[120:123], v[64:67]\n"
    "ds_read_b64_tr_b16 v[68:69], v16 offset:49152\n"
    "ds_read_b64_tr_b16 v[70:71], v16 offset:49664\n"
    "v_mfma_f32_16x16x32_bf16 v[60:63], v[72:75], v[124:127], v[60:63]\n"
    "ds_read_b128 v[80:83], v18 offset:864\n"
    "v_mfma_f32_16x16x32_bf16 v[56:59], v[72:75], v[128:131], v[56:59]\n"
    "ds_read_b128 v[84:87], v19 offset:5184\n"
    "v_mfma_f32_16x16x32_bf16 v[52:55], v[72:75], v[132:135], v[52:55]\n"
    "v_mfma_f32_16x16x32_bf16 v[48:51], v[72:75], v[136:139], v[48:51]\n"
    "v_mfma_f32_16x16x32_bf16 v[44:47], v[72:75], v[140:143], v[44:47]\n"
    "v_mfma_f32_16x16x32_bf16 v[40:43], v[72:75], v[148:151], v[40:43]\n"
    "v_mfma_f32_16x16x32_bf16 v[36:39], v[72:75], v[156:159], v[36:39]\n"
    "v_mfma_f32_16x16x32_bf16 v[32:35], v[72:75], v[164:167], v[32:35]\n"
    "s_waitcnt lgkmcnt(0)\n"
    "s_nop 0\n"
    "v_mfma_f32_16x16x32_bf16 v[64:67], v[68:71], v[124:127], v[64:67]\n"
    "ds_read_b64_tr_b16 v[72:73], v16 offset:53248\n"
    "ds_read_b64_tr_b16 v[74:75], v16 offset:53760\n"
    "v_mfma_f32_16x16x32_bf16 v[60:63], v[68:71], v[128:131], v[60:63]\n"
    "ds_read_b128 v[88:91], v18 offset:576\n"
    "v_mfma_f32_16x16x32_bf16 v[56:59], v[68:71], v[132:135], v[56:59]\n"
    "ds_read_b128 v[92:95], v19 offset:4896\n"
    "v_mfma_f32_16x16x32_bf16 v[52:55], v[68:71], v[136:139], v[52:55]\n"
    "v_mfma_f32_16x16x32_bf16 v[48:51], v[68:71], v[140:143], v[48:51]\n"
    "v_mfma_f32_16x16x32_bf16 v[44:47], v[68:71], v[148:151], v[44:47]\n"
    "v_mfma_f32_16x16x32_bf16 v[40:43], v[68:71], v[156:159], v[40:43]\n"
    "v_mfma_f32_16x16x32_bf16 v[36:39], v[68:71], v[164:167], v[36:39]\n"
    "v_mfma_f32_16x16x32_bf16 v[32:35], v[68:71], v[80:83], v[32:35]\n"
    "s_waitcnt lgkmcnt(0)\n"
    "s_nop 0\n"
    "v_mfma_f32_16x16x32_bf16 v[64:67], v[72:75], v[128:131], v[64:67]\n"
    "ds_read_b64_tr_b16 v[68:69], v16 offset:57344\n"
    "ds_read_b64_tr_b16 v[70:71], v16 offset:57856\n"
    "v_mfma_f32_16x16x32_bf16 v[60:63], v[72:75], v[132:135], v[60:63]\n"
    "ds_read_b128 v[96:99], v18 offset:288\n"
    "v_mfma_f32_16x16x32_bf16 v[56:59], v[72:75], v[136:139], v[56:59]\n"
    "ds_read_b128 v[100:103], v19 offset:4608\n"
    "v_mfma_f32_16x16x32_bf16 v[52:55], v[72:75], v[140:143], v[52:55]\n"
    "v_mfma_f32_16x16x32_bf16 v[48:51], v[72:75], v[148:151], v[48:51]\n"
    "v_mfma_f32_16x16x32_bf16 v[44:47], v[72:75], v[156:159], v[44:47]\n"
    "v_mfma_f32_16x16x32_bf16 v[40:43], v[72:75], v[164:167], v[40:43]\n"
    "v_mfma_f32_16x16x32_bf16 v[36:39], v[72:75], v[80:83], v[36:39]\n"
    "v_mfma_f32_16x16x32_bf16 v[32:35], v[72:75], v[88:91], v[32:35]\n"
    "s_waitcnt lgkmcnt(0)\n"
    "s_nop 0\n"
    "v_mfma_f32_16x16x32_bf16 v[64:67], v[68:71], v[132:135], v[64:67]\n"
    "ds_read_b64_tr_b16 v[72:73], v16 offset:61440\n"
    "ds_read_b64_tr_b16 v[74:75], v16 offset:61952\n"
    "v_mfma_f32_16x16x32_bf16 v[60:63], v[68:71], v[136:139], v[60:63]\n"
    "ds_read_b128 v[104:107], v18 offset:0\n"
    "v_mfma_f32_16x16x32_bf16 v[56:59], v[68:71], v[140:143], v[56:59]\n"
    "ds_read_b128 v[108:111], v19 offset:4320\n"
    "v_mfma_f32_16x16x32_bf16 v[52:55], v[68:71], v[148:151], v[52:55]\n"
    "v_mfma_f32_16x16x32_bf16 v[48:51], v[68:71], v[156:159], v[48:51]\n"
    "v_mfma_f32_16x16x32_bf16 v[44:47], v[68:71], v[164:167], v[44:47]\n"
    "v_mfma_f32_16x16x32_bf16 v[40:43], v[68:71], v[80:83], v[40:43]\n"
    "v_mfma_f32_16x16x32_bf16 v[36:39], v[68:71], v[88:91], v[36:39]\n"
    "v_mfma_f32_16x16x32_bf16 v[32:35], v[68:71], v[96:99], v[32:35]\n"
    "s_waitcnt lgkmcnt(0)\n"
    "s_barrier\n"
    "v_mfma_f32_16x16x32_bf16 v[64:67], v[72:75], v[136:139], v[64:67]\n"
    "ds_read_b64_tr_b16 v[68:69], v17 offset:0\n"
    "ds_read_b64_tr_b16 v[70:71], v17 offset:512\n"
    "v_mfma_f32_16x16x32_bf16 v[60:63], v[72:75], v[140:143], v[60:63]\n"
    "ds_read_b128 v[112:115], v19 offset:4032\n"
    "v_mfma_f32_16x16x32_bf16 v[56:59], v[72:75], v[148:151], v[56:59]\n"
    "v_mfma_f32_16x16x32_bf16 v[52:55], v[72:75], v[156:159], v[52:55]\n"
    "v_mfma_f32_16x16x32_bf16 v[48:51], v[72:75], v[164:167], v[48:51]\n"
    "v_mfma_f32_16x16x32_bf16 v[44:47], v[72:75], v[80:83], v[44:47]\n"
    "v_mfma_f32_16x16x32_bf16 v[40:43], v[72:75], v[88:91], v[40:43]\n"
    "v_mfma_f32_16x16x32_bf16 v[36:39], v[72:75], v[96:99], v[36:39]\n"
    "v_mfma_f32_16x16x32_bf16 v[32:35], v[72:75], v[104:107], v[32:35]\n"
    "s_waitcnt lgkmcnt(0)\n"
    "s_nop 0\n"
    "v_mfma_f32_16x16x32_bf16 v[64:67], v[68:71], v[144:147], v[64:67]\n"
    "ds_read_b64_tr_b16 v[72:73], v17 offset:4096\n"
    "ds_read_b64_tr_b16 v[74:75], v17 offset:4608\n"
    "v_mfma_f32_16x16x32_bf16 v[60:63], v[68:71], v[152:155], v[60:63]\n"
    "ds_read_b128 v[116:119], v19 offset:3744\n"
    "v_mfma_f32_16x16x32_bf16 v[56:59], v[68:71], v[160:163], v[56:59]\n"
    "v_mfma_f32_16x16x32_bf16 v[52:55], v[68:71], v[76:79], v[52:55]\n"
    "v_mfma_f32_16x16x32_bf16 v[48:51], v[68:71], v[84:87], v[48:51]\n"
    "v_mfma_f32_16x16x32_bf16 v[44:47], v[68:71], v[92:95], v[44:47]\n"
    "v_mfma_f32_16x16x32_bf16 v[40:43], v[68:71], v[100:103], v[40:43]\n"
    "v_mfma_f32_16x16x32_bf16 v[36:39], v[68:71], v[108:111], v[36:39]\n"
    "v_mfma_f32_16x16x32_bf16 v[32:35], v[68:71], v[112:115], v[32:35]\n"
    "s_waitcnt lgkmcnt(0)\n"
    "s_nop 0\n"
    "v_mfma_f32_16x16x32_bf16 v[64:67], v[72:75], v[152:155], v[64:67]\n"
    "ds_read_b64_tr_b16 v[68:69], v17 offset:8192\n"
    "ds_read_b64_tr_b16 v[70:71], v17 offset:8704\n"
    "v_mfma_f32_16x16x32_bf16 v[60:63], v[72:75], v[160:163], v[60:63]\n"
    "ds_read_b128 v[120:123], v19 offset:3456\n"
    "v_mfma_f32_16x16x32_bf16 v[56:59], v[72:75], v[76:79], v[56:59]\n"
    "v_mfma_f32_16x16x32_bf16 v[52:55], v[72:75], v[84:87], v[52:55]\n"
    "v_mfma_f32_16x16x32_bf16 v[48:51], v[72:75], v[92:95], v[48:51]\n"
    "v_mfma_f32_16x16x32_bf16 v[44:47], v[72:75], v[100:103], v[44:47]\n"
    "v_mfma_f32_16x16x32_bf16 v[40:43], v[72:75], v[108:111], v[40:43]\n"
    "v_mfma_f32_16x16x32_bf16 v[36:39], v[72:75], v[112:115], v[36:39]\n"
    "v_mfma_f32_16x16x32_bf16 v[32:35], v[72:75], v[116:119], v[32:35]\n"
    "s_waitcnt lgkmcnt(0)\n"
    "s_nop 0\n"
    "v_mfma_f32_16x16x32_bf16 v[64:67], v[68:71], v[160:163], v[64:67]\n"
    "ds_read_b64_tr_b16 v[72:73], v17 offset:12288\n"
    "ds_read_b64_tr_b16 v[74:75], v17 offset:12800\n"
    "v_mfma_f32_16x16x32_bf16 v[60:63], v[68:71], v[76:79], v[60:63]\n"
    "ds_read_b128 v[124:127], v19 offset:3168\n"
    "v_mfma_f32_16x16x32_bf16 v[56:59], v[68:71], v[84:87], v[56:59]\n"
    "v_mfma_f32_16x16x32_bf16 v[52:55], v[68:71], v[92:95], v[52:55]\n"
    "v_mfma_f32_16x16x32_bf16 v[48:51], v[68:71], v[100:103], v[48:51]\n"
    "v_mfma_f32_16x16x32_bf16 v[44:47], v[68:71], v[108:111], v[44:47]\n"
    "v_mfma_f32_16x16x32_bf16 v[40:43], v[68:71], v[112:115], v[40:43]\n"
    "v_mfma_f32_16x16x32_bf16 v[36:39], v[68:71], v[116:119], v[36:39]\n"
    "v_mfma_f32_16x16x32_bf16 v[32:35], v[68:71], v[120:123], v[32:35]\n"
    "s_waitcnt lgkmcnt(0)\n"
    "s_barrier\n"
    "v_mfma_f32_16x16x32_bf16 v[64:67], v[72:75], v[76:79], v[64:67]\n"
    "ds_read_b64_tr_b16 v[68:69], v17 offset:16384\n"
    "ds_read_b64_tr_b16 v[70:71], v17 offset:16896\n"
    "v_mfma_f32_16x16x32_bf16 v[60:63], v[72:75], v[84:87], v[60:63]\n"
    "ds_read_b128 v[128:131], v19 offset:2880\n"
    "v_mfma_f32_16x16x32_bf16 v[56:59], v[72:75], v[92:95], v[56:59]\n"
    "v_mfma_f32_16x16x32_bf16 v[52:55], v[72:75], v[100:103], v[52:55]\n"
    "v_mfma_f32_16x16x32_bf16 v[48:51], v[72:75], v[108:111], v[48:51]\n"
    "v_mfma_f32_16x16x32_bf16 v[44:47], v[72:75], v[112:115], v[44:47]\n"
    "v_mfma_f32_16x16x32_bf16 v[40:43], v[72:75], v[116:119], v[40:43]\n"
    "v_mfma_f32_16x16x32_bf16 v[36:39], v[72:75], v[120:123], v[36:39]\n"
    "v_mfma_f32_16x16x32_bf16 v[32:35], v[72:75], v[124:127], v[32:35]\n"
    "s_waitcnt lgkmcnt(0)\n"
    "s_nop 0\n"
    "v_mfma_f32_16x16x32_bf16 v[64:67], v[68:71], v[84:87], v[64:67]\n"
    "ds_read_b64_tr_b16 v[72:73], v17 offset:20480\n"
    "ds_read_b64_tr_b16 v[74:75], v17 offset:20992\n"
    "v_mfma_f32_16x16x32_bf16 v[60:63], v[68:71], v[92:95], v[60:63]\n"
    "ds_read_b128 v[132:135], v19 offset:2592\n"
    "v_mfma_f32_16x16x32_bf16 v[56:59], v[68:71], v[100:103], v[56:59]\n"
    "v_mfma_f32_16x16x32_bf16 v[52:55], v[68:71], v[108:111], v[52:55]\n"
    "v_mfma_f32_16x16x32_bf16 v[48:51], v[68:71], v[112:115], v[48:51]\n"
    "v_mfma_f32_16x16x32_bf16 v[44:47], v[68:71], v[116:119], v[44:47]\n"
    "v_mfma_f32_16x16x32_bf16 v[40:43], v[68:71], v[120:123], v[40:43]\n"
    "v_mfma_f32_16x16x32_bf16 v[36:39], v[68:71], v[124:127], v[36:39]\n"
    "v_mfma_f32_16x16x32_bf16 v[32:35], v[68:71], v[128:131], v[32:35]\n"
    "s_waitcnt lgkmcnt(0)\n"
    "s_nop 0\n"
    "v_mfma_f32_16x16x32_bf16 v[64:67], v[72:75], v[92:95], v[64:67]\n"
    "ds_read_b64_tr_b16 v[68:69], v17 offset:24576\n"
    "ds_read_b64_tr_b16 v[70:71], v17 offset:25088\n"
    "v_mfma_f32_16x16x32_bf16 v[60:63], v[72:75], v[100:103], v[60:63]\n"
    "ds_read_b128 v[136:139], v19 offset:2304\n"
    "v_mfma_f32_16x16x32_bf16 v[56:59], v[72:75], v[108:111], v[56:59]\n"
    "v_mfma_f32_16x16x32_bf16 v[52:55], v[72:75], v[112:115], v[52:55]\n"
    "v_mfma_f32_16x16x32_bf16 v[48:51], v[72:75], v[116:119], v[48:51]\n"
    "v_mfma_f32_16x16x32_bf16 v[44:47], v[72:75], v[120:123], v[44:47]\n"
    "v_mfma_f32_16x16x32_bf16 v[40:43], v[72:75], v[124:127], v[40:43]\n"
    "v_mfma_f32_16x16x32_bf16 v[36:39], v[72:75], v[128:131], v[36:39]\n"
    "v_mfma_f32_16x16x32_bf16 v[32:35], v[72:75], v[132:135], v[32:35]\n"
    "s_waitcnt lgkmcnt(0)\n"
    "s_nop 0\n"
    "v_mfma_f32_16x16x32_bf16 v[64:67], v[68:71], v[100:103], v[64:67]\n"
    "ds_read_b64_tr_b16 v[72:73], v17 offset:28672\n"
    "ds_read_b64_tr_b16 v[74:75], v17 offset:29184\n"
    "v_mfma_f32_16x16x32_bf16 v[60:63], v[68:71], v[108:111], v[60:63]\n"
    "ds_read_b128 v[140:143], v19 offset:2016\n"
    "v_mfma_f32_16x16x32_bf16 v[56:59], v[68:71], v[112:115], v[56:59]\n"
    "ds_read_b128 v[148:151], v20 offset:5760\n"
    "v_mfma_f32_16x16x32_bf16 v[52:55], v[68:71], v[116:119], v[52:55]\n"
    "v_mfma_f32_16x16x32_bf16 v[48:51], v[68:71], v[120:123], v[48:51]\n"
    "v_mfma_f32_16x16x32_bf16 v[44:47], v[68:71], v[124:127], v[44:47]\n"
    "v_mfma_f32_16x16x32_bf16 v[40:43], v[68:71], v[128:131], v[40:43]\n"
    "v_mfma_f32_16x16x32_bf16 v[36:39], v[68:71], v[132:135], v[36:39]\n"
    "v_mfma_f32_16x16x32_bf16 v[32:35], v[68:71], v[136:139], v[32:35]\n"
    "s_waitcnt lgkmcnt(0)\n"
    "s_nop 0\n"
    "v_mfma_f32_16x16x32_bf16 v[64:67], v[72:75], v[108:111], v[64:67]\n"
    "ds_read_b64_tr_b16 v[68:69], v17 offset:32768\n"
    "ds_read_b64_tr_b16 v[70:71], v17 offset:33280\n"
    "v_mfma_f32_16x16x32_bf16 v[60:63], v[72:75], v[112:115], v[60:63]\n"
    "ds_read_b128 v[156:159], v19 offset:1728\n"
    "v_mfma_f32_16x16x32_bf16 v[56:59], v[72:75], v[116:119], v[56:59]\n"
    "ds_read_b128 v[164:167], v20 offset:5472\n"
    "v_mfma_f32_16x16x32_bf16 v[52:55], v[72:75], v[120:123], v[52:55]\n"
    "v_mfma_f32_16x16x32_bf16 v[48:51], v[72:75], v[124:127], v[48:51]\n"
    "v_mfma_f32_16x16x32_bf16 v[44:47], v[72:75], v[128:131], v[44:47]\n"
    "v_mfma_f32_16x16x32_bf16 v[40:43], v[72:75], v[132:135], v[40:43]\n"
    "v_mfma_f32_16x16x32_bf16 v[36:39], v[72:75], v[136:139], v[36:39]\n"
    "v_mfma_f32_16x16x32_bf16 v[32:35], v[72:75], v[140:143], v[32:35]\n"
    "s_waitcnt lgkmcnt(0)\n"
    "s_nop 0\n"
    "v_mfma_f32_16x16x32_bf16 v[64:67], v[68:71], v[112:115], v[64:67]\n"
    "ds_read_b64_tr_b16 v[72:73], v17 offset:36864\n"
    "ds_read_b64_tr_b16 v[74:75], v17 offset:37376\n"
    "v_mfma_f32_16x16x32_bf16 v[60:63], v[68:71], v[116:119], v[60:63]\n"
    "ds_read_b128 v[80:83], v19 offset:1440\n"
    "v_mfma_f32_16x16x32_bf16 v[56:59], v[68:71], v[120:123], v[56:59]\n"
    "ds_read_b128 v[88:91], v20 offset:5184\n"
    "v_mfma_f32_16x16x32_bf16 v[52:55], v[68:71], v[124:127], v[52:55]\n"
    "v_mfma_f32_16x16x32_bf16 v[48:51], v[68:71], v[128:131], v[48:51]\n"
    "v_mfma_f32_16x16x32_bf16 v[44:47], v[68:71], v[132:135], v[44:47]\n"
    "v_mfma_f32_16x16x32_bf16 v[40:43], v[68:71], v[136:139], v[40:43]\n"
    "v_mfma_f32_16x16x32_bf16 v[36:39], v[68:71], v[140:143], v[36:39]\n"
    "v_mfma_f32_16x16x32_bf16 v[32:35], v[68:71], v[156:159], v[32:35]\n"
    "s_waitcnt lgkmcnt(0)\n"
    "s_nop 0\n"
    "v_mfma_f32_16x16x32_bf16 v[64:67], v[72:75], v[116:119], v[64:67]\n"
    "ds_read_b64_tr_b16 v[68:69], v17 offset:40960\n"
    "ds_read_b64_tr_b16 v[70:71], v17 offset:41472\n"
    "v_mfma_f32_16x16x32_bf16 v[60:63], v[72:75], v[120:123], v[60:63]\n"
    "ds_read_b128 v[96:99], v19 offset:1152\n"
    "v_mfma_f32_16x16x32_bf16 v[56:59], v[72:75], v[124:127], v[56:59]\n"
    "ds_read_b128 v[104:107], v20 offset:4896\n"
    "v_mfma_f32_16x16x32_bf16 v[52:55], v[72:75], v[128:131], v[52:55]\n"
    "v_mfma_f32_16x16x32_bf16 v[48:51], v[72:75], v[132:135], v[48:51]\n"
    "v_mfma_f32_16x16x32_bf16 v[44:47], v[72:75], v[136:139], v[44:47]\n"
    "v_mfma_f32_16x16x32_bf16 v[40:43], v[72:75], v[140:143], v[40:43]\n"
    "v_mfma_f32_16x16x32_bf16 v[36:39], v[72:75], v[156:159], v[36:39]\n"
    "v_mfma_f32_16x16x32_bf16 v[32:35], v[72:75], v[80:83], v[32:35]\n"
    "s_waitcnt lgkmcnt(0)\n"
    "s_barrier\n"
    "v_mfma_f32_16x16x32_bf16 v[64:67], v[68:71], v[120:123], v[64:67]\n"
    "ds_read_b64_tr_b16 v[72:73], v16 offset:0\n"
    "ds_read_b64_tr_b16 v[74:75], v16 offset:512\n"
    "v_mfma_f32_16x16x32_bf16 v[60:63], v[68:71], v[124:127], v[60:63]\n"
    "ds_read_b128 v[144:147], v19 offset:864\n"
    "v_mfma_f32_16x16x32_bf16 v[56:59], v[68:71], v[128:131], v[56:59]\n"
    "ds_read_b128 v[152:155], v20 offset:4608\n"
    "v_mfma_f32_16x16x32_bf16 v[52:55], v[68:71], v[132:135], v[52:55]\n"
    "v_mfma_f32_16x16x32_bf16 v[48:51], v[68:71], v[136:139], v[48:51]\n"
    "v_mfma_f32_16x16x32_bf16 v[44:47], v[68:71], v[140:143], v[44:47]\n"
    "v_mfma_f32_16x16x32_bf16 v[40:43], v[68:71], v[156:159], v[40:43]\n"
    "v_mfma_f32_16x16x32_bf16 v[36:39], v[68:71], v[80:83], v[36:39]\n"
    "v_mfma_f32_16x16x32_bf16 v[32:35], v[68:71], v[96:99], v[32:35]\n"
    "s_waitcnt lgkmcnt(0)\n"
    "s_nop 0\n"
    "v_mfma_f32_16x16x32_bf16 v[64:67], v[72:75], v[124:127], v[64:67]\n"
    "ds_read_b64_tr_b16 v[160:161], v16 offset:4096\n"
    "ds_read_b64_tr_b16 v[162:163], v16 offset:4608\n"
    "v_mfma_f32_16x16x32_bf16 v[60:63], v[72:75], v[128:131], v[60:63]\n"
    "ds_read_b64_tr_b16 v[76:77], v16 offset:8192\n"
    "ds_read_b64_tr_b16 v[78:79], v16 offset:8704\n"
    "v_mfma_f32_16x16x32_bf16 v[56:59], v[72:75], v[132:135], v[56:59]\n"
    "ds_read_b64_tr_b16 v[84:85], v16 offset:12288\n"
    "ds_read_b64_tr_b16 v[86:87], v16 offset:12800\n"
    "v_mfma_f32_16x16x32_bf16 v[52:55], v[72:75], v[136:139], v[52:55]\n"
    "v_mfma_f32_16x16x32_bf16 v[48:51], v[72:75], v[140:143], v[48:51]\n"
    "s_waitcnt lgkmcnt(4)\n"
    "s_nop 0\n"
    "v_mfma_f32_16x16x32_bf16 v[64:67], v[160:163], v[128:131], v[64:67]\n"
    "v_mfma_f32_16x16x32_bf16 v[60:63], v[160:163], v[132:135], v[60:63]\n"
    "ds_read_b64_tr_b16 v[92:93], v16 offset:16384\n"
    "ds_read_b64_tr_b16 v[94:95], v16 offset:16896\n"
    "v_mfma_f32_16x16x32_bf16 v[56:59], v[160:163], v[136:139], v[56:59]\n"
    "v_mfma_f32_16x16x32_bf16 v[52:55], v[160:163], v[140:143], v[52:55]\n"
    "v_mfma_f32_16x16x32_bf16 v[48:51], v[160:163], v[156:159], v[48:51]\n"
    "s_waitcnt lgkmcnt(2)\n"
    "s_nop 0\n"
    "v_mfma_f32_16x16x32_bf16 v[64:67], v[76:79], v[132:135], v[64:67]\n"
    "v_mfma_f32_16x16x32_bf16 v[60:63], v[76:79], v[136:139], v[60:63]\n"
    "ds_read_b64_tr_b16 v[100:101], v16 offset:20480\n"
    "ds_read_b64_tr_b16 v[102:103], v16 offset:20992\n"
    "v_mfma_f32_16x16x32_bf16 v[56:59], v[76:79], v[140:143], v[56:59]\n"
    "ds_read_b128 v[108:111], v20 offset:4320\n"
    "v_mfma_f32_16x16x32_bf16 v[52:55], v[76:79], v[156:159], v[52:55]\n"
    "ds_read_b128 v[112:115], v20 offset:4032\n"
    "v_mfma_f32_16x16x32_bf16 v[48:51], v[76:79], v[80:83], v[48:51]\n"
    "ds_read_b128 v[116:119], v20 offset:3744\n"
    "v_mfma_f32_16x16x32_bf16 v[64:67], v[84:87], v[136:139], v[64:67]\n"
    "ds_read_b128 v[120:123], v20 offset:3456\n"
    "v_mfma_f32_16x16x32_bf16 v[60:63], v[84:87], v[140:143], v[60:63]\n"
    "ds_read_b64_tr_b16 v[68:69], v16 offset:24576\n"
    "ds_read_b64_tr_b16 v[70:71], v16 offset:25088\n"
    "v_mfma_f32_16x16x32_bf16 v[56:59], v[84:87], v[156:159], v[56:59]\n"
    "ds_read_b128 v[124:127], v20 offset:3168\n"
    "v_mfma_f32_16x16x32_bf16 v[52:55], v[84:87], v[80:83], v[52:55]\n"
    "ds_read_b128 v[72:75], v20 offset:2880\n"
    "v_mfma_f32_16x16x32_bf16 v[48:51], v[84:87], v[96:99], v[48:51]\n"
    "ds_read_b128 v[128:131], v20 offset:2592\n"
    "s_waitcnt lgkmcnt(6)\n"
    "s_nop 0\n"
    "v_mfma_f32_16x16x32_bf16 v[64:67], v[92:95], v[148:151], v[64:67]\n"
    "ds_read_b128 v[160:163], v20 offset:2304\n"
    "v_mfma_f32_16x16x32_bf16 v[60:63], v[92:95], v[164:167], v[60:63]\n"
    "ds_read_b64_tr_b16 v[132:133], v16 offset:28672\n"
    "ds_read_b64_tr_b16 v[134:135], v16 offset:29184\n"
    "v_mfma_f32_16x16x32_bf16 v[56:59], v[92:95], v[88:91], v[56:59]\n"
    "ds_read_b128 v[76:79], v20 offset:2016\n"
    "v_mfma_f32_16x16x32_bf16 v[52:55], v[92:95], v[104:107], v[52:55]\n"
    "ds_read_b128 v[136:139], v20 offset:1728\n"
    "v_mfma_f32_16x16x32_bf16 v[48:51], v[92:95], v[152:155], v[48:51]\n"
    "ds_read_b128 v[140:143], v20 offset:1440\n"
    "v_mfma_f32_16x16x32_bf16 v[64:67], v[100:103], v[152:155], v[64:67]\n"
    "ds_read_b128 v[84:87], v20 offset:1152\n"
    "v_mfma_f32_16x16x32_bf16 v[60:63], v[100:103], v[108:111], v[60:63]\n"
    "ds_read_b64_tr_b16 v[148:149], v16 offset:0\n"
    "ds_read_b64_tr_b16 v[150:151], v16 offset:512\n"
    "v_mfma_f32_16x16x32_bf16 v[56:59], v[100:103], v[112:115], v[56:59]\n"
    "v_mfma_f32_16x16x32_bf16 v[52:55], v[100:103], v[116:119], v[52:55]\n"
    "s_waitcnt lgkmcnt(4)\n"
    "s_nop 0\n"
    "v_mfma_f32_16x16x32_bf16 v[48:51], v[100:103], v[120:123], v[48:51]\n"
    "v_mfma_f32_16x16x32_bf16 v[64:67], v[68:71], v[120:123], v[64:67]\n"
    "ds_read_b64_tr_b16 v[164:165], v16 offset:4096\n"
    "ds_read_b64_tr_b16 v[166:167], v16 offset:4608\n"
    "v_mfma_f32_16x16x32_bf16 v[60:63], v[68:71], v[124:127], v[60:63]\n"
    "v_mfma_f32_16x16x32_bf16 v[56:59], v[68:71], v[72:75], v[56:59]\n"
    "v_mfma_f32_16x16x32_bf16 v[52:55], v[68:71], v[128:131], v[52:55]\n"
    "ds_read_b128 v[88:91], v19 offset:576\n"
    "v_mfma_f32_16x16x32_bf16 v[48:51], v[68:71], v[160:163], v[48:51]\n"
    "ds_read_b64_tr_b16 v[104:105], v16 offset:8192\n"
    "ds_read_b64_tr_b16 v[106:107], v16 offset:8704\n"
    "v_mfma_f32_16x16x32_bf16 v[64:67], v[132:135], v[160:163], v[64:67]\n"
    "v_mfma_f32_16x16x32_bf16 v[60:63], v[132:135], v[76:79], v[60:63]\n"
    "v_mfma_f32_16x16x32_bf16 v[56:59], v[132:135], v[136:139], v[56:59]\n"
    "ds_read_b128 v[92:95], v19 offset:288\n"
    "s_waitcnt lgkmcnt(4)\n"
    "s_nop 0\n"
    "v_mfma_f32_16x16x32_bf16 v[52:55], v[132:135], v[140:143], v[52:55]\n"
    "ds_read_b64_tr_b16 v[152:153], v16 offset:12288\n"
    "ds_read_b64_tr_b16 v[154:155], v16 offset:12800\n"
    "v_mfma_f32_16x16x32_bf16 v[48:51], v[132:135], v[84:87], v[48:51]\n"
    "v_mfma_f32_16x16x32_bf16 v[44:47], v[148:151], v[156:159], v[44:47]\n"
    "v_mfma_f32_16x16x32_bf16 v[40:43], v[148:151], v[80:83], v[40:43]\n"
    "ds_read_b128 v[100:103], v19 offset:0\n"
    "v_mfma_f32_16x16x32_bf16 v[36:39], v[148:151], v[96:99], v[36:39]\n"
    "v_cvt_pk_bf16_f32 v64, v64, v65\n"
    "v_cvt_pk_bf16_f32 v65, v66, v67\n"
    "ds_read_b64_tr_b16 v[68:69], v16 offset:16384\n"
    "ds_read_b64_tr_b16 v[70:71], v16 offset:16896\n"
    "v_mfma_f32_16x16x32_bf16 v[32:35], v[148:151], v[144:147], v[32:35]\n"
    "buffer_store_dwordx2 v[64:65], v26, s[32:35], 0 offen sc1\n"
    "v_cvt_pk_bf16_f32 v56, v56, v57\n"
    "v_mfma_f32_16x16x32_bf16 v[44:47], v[164:167], v[80:83], v[44:47]\n"
    "v_cvt_pk_bf16_f32 v57, v58, v59\n"
    "v_cvt_pk_bf16_f32 v58, v60, v61\n"
    "v_mfma_f32_16x16x32_bf16 v[40:43], v[164:167], v[96:99], v[40:43]\n"
    "v_cvt_pk_bf16_f32 v59, v62, v63\n"
    "buffer_store_dwordx4 v[56:59], v25, s[32:35], 0 offen offset:2304 sc1\n"
    "v_mfma_f32_16x16x32_bf16 v[36:39], v[164:167], v[144:147], v[36:39]\n"
    "v_cvt_pk_bf16_f32 v48, v48, v49\n"
    "v_cvt_pk_bf16_f32 v49, v50, v51\n"
    "ds_read_b64_tr_b16 v[132:133], v16 offset:20480\n"
    "ds_read_b64_tr_b16 v[134:135], v16 offset:20992\n"
    "s_waitcnt lgkmcnt(4)\n"
    "s_nop 0\n"
    "v_mfma_f32_16x16x32_bf16 v[32:35], v[164:167], v[88:91], v[32:35]\n"
    "v_cvt_pk_bf16_f32 v50, v52, v53\n"
    "v_cvt_pk_bf16_f32 v51, v54, v55\n"
    "v_mfma_f32_16x16x32_bf16 v[44:47], v[104:107], v[96:99], v[44:47]\n"
    "buffer_store_dwordx4 v[48:51], v25, s[32:35], 0 offen sc1\n"
    "v_mfma_f32_16x16x32_bf16 v[40:43], v[104:107], v[144:147], v[40:43]\n"
    "v_mfma_f32_16x16x32_bf16 v[36:39], v[104:107], v[88:91], v[36:39]\n"
    "ds_read_b64_tr_b16 v[156:157], v16 offset:24576\n"
    "ds_read_b64_tr_b16 v[158:159], v16 offset:25088\n"
    "v_mfma_f32_16x16x32_bf16 v[32:35], v[104:107], v[92:95], v[32:35]\n"
    "v_mfma_f32_16x16x32_bf16 v[44:47], v[152:155], v[144:147], v[44:47]\n"
    "v_mfma_f32_16x16x32_bf16 v[40:43], v[152:155], v[88:91], v[40:43]\n"
    "v_mfma_f32_16x16x32_bf16 v[36:39], v[152:155], v[92:95], v[36:39]\n"
    "ds_read_b64_tr_b16 v[148:149], v16 offset:28672\n"
    "ds_read_b64_tr_b16 v[150:151], v16 offset:29184\n"
    "v_mfma_f32_16x16x32_bf16 v[32:35], v[152:155], v[100:103], v[32:35]\n"
    "ds_read_b128 v[80:83], v20 offset:864\n"
    "s_waitcnt lgkmcnt(3)\n"
    "s_nop 0\n"
    "v_mfma_f32_16x16x32_bf16 v[44:47], v[68:71], v[108:111], v[44:47]\n"
    "ds_read_b128 v[164:167], v20 offset:576\n"
    "v_mfma_f32_16x16x32_bf16 v[40:43], v[68:71], v[112:115], v[40:43]\n"
    "ds_read_b128 v[96:99], v20 offset:288\n"
    "v_mfma_f32_16x16x32_bf16 v[36:39], v[68:71], v[116:119], v[36:39]\n"
    "ds_read_b128 v[104:107], v20 offset:0\n"
    "v_mfma_f32_16x16x32_bf16 v[32:35], v[68:71], v[120:123], v[32:35]\n"
    "v_mfma_f32_16x16x32_bf16 v[44:47], v[132:135], v[124:127], v[44:47]\n"
    "v_mfma_f32_16x16x32_bf16 v[40:43], v[132:135], v[72:75], v[40:43]\n"
    "v_mfma_f32_16x16x32_bf16 v[36:39], v[132:135], v[128:131], v[36:39]\n"
    "v_mfma_f32_16x16x32_bf16 v[32:35], v[132:135], v[160:163], v[32:35]\n"
    "v_mfma_f32_16x16x32_bf16 v[44:47], v[156:159], v[76:79], v[44:47]\n"
    "v_mfma_f32_16x16x32_bf16 v[40:43], v[156:159], v[136:139], v[40:43]\n"
    "v_mfma_f32_16x16x32_bf16 v[36:39], v[156:159], v[140:143], v[36:39]\n"
    "v_mfma_f32_16x16x32_bf16 v[32:35], v[156:159], v[84:87], v[32:35]\n"
    "s_waitcnt lgkmcnt(0)\n"
    "s_nop 0\n"
    "v_mfma_f32_16x16x32_bf16 v[44:47], v[148:151], v[80:83], v[44:47]\n"
    "v_mfma_f32_16x16x32_bf16 v[40:43], v[148:151], v[164:167], v[40:43]\n"
    "v_mfma_f32_16x16x32_bf16 v[36:39], v[148:151], v[96:99], v[36:39]\n"
    "v_mfma_f32_16x16x32_bf16 v[32:35], v[148:151], v[104:107], v[32:35]\n"
    "s_nop 2\n"
    "v_cvt_pk_bf16_f32 v146, v44, v45\n"
    "v_cvt_pk_bf16_f32 v147, v46, v47\n"
    "v_cvt_pk_bf16_f32 v144, v40, v41\n"
    "v_cvt_pk_bf16_f32 v145, v42, v43\n"
    "buffer_store_dwordx4 v[144:147], v24, s[32:35], 0 offen offset:2304 sc1\n"
    "v_cvt_pk_bf16_f32 v90, v36, v37\n"
    "v_cvt_pk_bf16_f32 v91, v38, v39\n"
    "v_cvt_pk_bf16_f32 v88, v32, v33\n"
    "v_cvt_pk_bf16_f32 v89, v34, v35\n"
    "buffer_store_dwordx4 v[88:91], v24, s[32:35], 0 offen sc1\n"
    "s_call_b64 s[44:45], .Lwn_p2\n"
    ".Lwn_ph2:\n"
    "s_barrier\n"
    "s_sleep 13\n"
    "buffer_load_dwordx4 v[32:35], v1, s[32:35], 0 offen sc1\n"
    "buffer_load_dwordx4 v[36:39], v1, s[32:35], 0 offen offset:576 sc1\n"
    "ds_read_b64_tr_b16 v[48:49], v2 offset:0\n"
    "ds_read_b64_tr_b16 v[50:51], v2 offset:1152\n"
    "ds_read_b64_tr_b16 v[52:53], v2 offset:9216\n"
    "ds_read_b64_tr_b16 v[54:55], v2 offset:10368\n"
    "ds_read_b64_tr_b16 v[56:57], v2 offset:18432\n"
    "ds_read_b64_tr_b16 v[58:59], v2 offset:19584\n"
    "ds_read_b64_tr_b16 v[60:61], v2 offset:27648\n"
    "ds_read_b64_tr_b16 v[62:63], v2 offset:28800\n"
    "ds_read_b64_tr_b16 v[64:65], v3\n"
    "s_mov_b32 s4, 0x40000\n"
    ".Lwn_poll:\n"
    "s_waitcnt vmcnt(0)\n"
    "v_max_u32 v40, v32, v33\n"
    "v_max3_u32 v41, v34, v35, v36\n"
    "v_max3_u32 v40, v37, v38, v40\n"
    "v_max3_u32 v40, v39, v41, v40\n"
    "v_cmp_eq_u32 vcc, -1, v40\n"
    "s_cbranch_vccnz .Lwn_again\n"
    ".Lwn_got:\n"
    "v_mfma_f32_16x16x32_bf16 v[40:43], v[8:11], v[32:35], 0\n"
    "v_mfma_f32_16x16x32_bf16 v[40:43], v[12:15], v[36:39], v[40:43]\n"
    "s_nop 7\n"
    "v_mul_f32 v40, 0x4038aa3b, v40\n"
    "v_mul_f32 v41, 0x4038aa3b, v41\n"
    "v_mul_f32 v42, 0xbfb8aa3b, v42\n"
    "v_mul_f32 v43, 0xbfb8aa3b, v43\n"
    "v_exp_f32 v40, v40\n"
    "v_exp_f32 v41, v41\n"
    "v_exp_f32 v42, v42\n"
    "v_exp_f32 v43, v43\n"
    "v_add_f32 v40, 1.0, v40\n"
    "v_add_f32 v41, 1.0, v41\n"
    "v_add_f32 v42, 1.0, v42\n"
    "v_add_f32 v43, 1.0, v43\n"
    "v_rcp_f32 v40, v40\n"
    "v_rcp_f32 v41, v41\n"
    "v_rcp_f32 v42, v42\n"
    "v_rcp_f32 v43, v43\n"
    "v_fma_f32 v40, v40, -2.0, 1.0\n"
    "v_fma_f32 v41, v41, -2.0, 1.0\n"
    "v_mul_f32 v40, v40, v42\n"
    "v_mul_f32 v41, v41, v43\n"
    "v_cvt_pk_bf16_f32 v40, v40, v41\n"
    "ds_write_b32 v4, v40\n"
    "s_waitcnt lgkmcnt(0)\n"
    "s_barrier\n"
    "ds_read_b128 v[68:71], v5\n"
    "ds_read_b128 v[72:75], v5 offset:64\n"
    "ds_read_b128 v[76:79], v5 offset:128\n"
    "ds_read_b128 v[80:83], v5 offset:192\n"
    "ds_read_b64 v[84:85], v6\n"
    "s_waitcnt lgkmcnt(4)\n"
    "v_mfma_f32_16x16x32_bf16 v[32:35], v[48:51], v[68:71], 0\n"
    "s_waitcnt lgkmcnt(3)\n"
    "v_mfma_f32_16x16x32_bf16 v[36:39], v[52:55], v[72:75], 0\n"
    "s_waitcnt lgkmcnt(2)\n"
    "v_mfma_f32_16x16x32_bf16 v[32:35], v[56:59], v[76:79], v[32:35]\n"
    "s_waitcnt lgkmcnt(1)\n"
    "v_mfma_f32_16x16x32_bf16 v[36:39], v[60:63], v[80:83], v[36:39]\n"
    "s_waitcnt lgkmcnt(0)\n"
    "v_mfma_f32_16x16x16_bf16 v[40:43], v[64:65], v[84:85], 0\n"
    "s_add_u32 s4, s13, s12\n"
    "s_cmp_eq_u32 s13, 8\n"
    "s_cselect_b32 s4, s4, 0\n"
    "s_nop 4\n"
    "v_pk_add_f32 v[32:33], v[32:33], v[36:37]\n"
    "v_pk_add_f32 v[34:35], v[34:35], v[38:39]\n"
    "v_pk_add_f32 v[32:33], v[32:33], v[40:41]\n"
    "v_pk_add_f32 v[34:35], v[34:35], v[42:43]\n"
    "s_cmp_eq_u32 s4, 8\n"
    "v_cvt_pk_bf16_f32 v32, v32, v33\n"
    "v_cvt_pk_bf16_f32 v33, v34, v35\n"
    "buffer_store_dwordx2 v[32:33], v7, s[28:31], 0 offen sc0 sc1\n"
    "s_cbranch_scc0 .Lwn_fin\n"
    "s_mov_b64 exec, 1\n"
    "buffer_store_dword v30, off, s[32:35], s58 sc1\n"
    "s_mov_b64 exec, -1\n"
    ".Lwn_fin:\n"
    "s_endpgm\n"
    ".Lwn_again:\n"
    "s_sub_u32 s4, s4, 1\n"
    "s_cbranch_scc1 .Lwn_got\n"
    "s_sleep 8\n"
    "buffer_load_dwordx4 v[32:35], v1, s[32:35], 0 offen sc1\n"
    "buffer_load_dwordx4 v[36:39], v1, s[32:35], 0 offen offset:576 sc1\n"
    "s_branch .Lwn_poll\n"
    ".Lwn_ld2:\n"
    "s_call_b64 s[44:45], .Lwn_p2\n"
    "s_mul_i32 s53, s6, 1290240\n"
    "s_add_u32 s53, s53, 36864\n"
    "s_lshl_b32 s4, s6, 4\n"
    "s_add_u32 s4, s4, s50\n"
    "s_mul_i32 s4, s4, 72\n"
    "s_add_u32 s4, s4, 64\n"
    "s_mul_i32 s4, s4, 1152\n"
    "v_add_u32 v17, s4, v21\n"
    "s_mov_b32 s26, 41472\n"
    "s_mov_b32 s27, 0x20000\n"
    "s_lshl_b32 s4, s50, 12\n"
    "v_lshl_add_u32 v18, v28, 4, s4\n"
    "s_add_u32 m0, m0, 0x1000\n"
    "s_add_u32 s51, s51, 82944\n"
    "buffer_load_dwordx4 v16, s[20:23], s51 offen lds\n"
    "s_add_u32 m0, m0, 0x1000\n"
    "s_add_u32 s51, s51, 82944\n"
    "buffer_load_dwordx4 v16, s[20:23], s51 offen lds\n"
    "s_add_u32 m0, m0, 0x1000\n"
    "s_add_u32 s51, s51, 82944\n"
    "buffer_load_dwordx4 v16, s[20:23], s51 offen lds\n"
    "s_add_u32 m0, m0, 0x1000\n"
    "s_add_u32 s51, s51, 82944\n"
    "buffer_load_dwordx4 v16, s[20:23], s51 offen lds\n"
    "s_add_u32 m0, m0, 0x1000\n"
    "s_add_u32 s51, s51, 82944\n"
    "buffer_load_dwordx4 v16, s[20:23], s51 offen lds\n"
    "s_waitcnt vmcnt(5)\n"
    "s_barrier\n"
    "s_add_u32 m0, m0, 0x1000\n"
    "s_add_u32 s51, s51, 82944\n"
    "buffer_load_dwordx4 v16, s[20:23], s51 offen lds\n"
    "s_add_u32 m0, m0, 0x1000\n"
    "s_add_u32 s51, s51, 82944\n"
    "buffer_load_dwordx4 v16, s[20:23], s51 offen lds\n"
    "s_add_u32 m0, m0, 0x1000\n"
    "s_add_u32 s51, s51, 82944\n"
    "buffer_load_dwordx4 v16, s[20:23], s51 offen lds\n"
    "s_add_u32 m0, m0, 0x1000\n"
    "s_add_u32 s51, s51, 82944\n"
    "buffer_load_dwordx4 v16, s[20:23], s51 offen lds\n"
    "s_waitcnt vmcnt(7)\n"
    "s_barrier\n"
    "s_add_u32 m0, m0, 0x1000\n"
    "s_add_u32 s51, s51, 82944\n"
    "buffer_load_dwordx4 v16, s[20:23], s51 offen lds\n"
    "s_add_u32 m0, m0, 0x1000\n"
    "s_add_u32 s51, s51, 82944\n"
    "buffer_load_dwordx4 v16, s[20:23], s51 offen lds\n"
    "s_add_u32 m0, m0, 0x1000\n"
    "s_add_u32 s51, s51, 82944\n"
    "buffer_load_dwordx4 v16, s[20:23], s51 offen lds\n"
    "s_add_u32 m0, m0, 0x1000\n"
    "s_add_u32 s51, s51, 82944\n"
    "buffer_load_dwordx4 v16, s[20:23], s51 offen lds\n"
    "s_add_u32 m0, m0, 0x1000\n"
    "s_add_u32 s51, s51, 82944\n"
    "buffer_load_dwordx4 v16, s[20:23], s51 offen lds\n"
    "s_waitcnt vmcnt(9)\n"
    "s_barrier\n"
    "s_add_u32 m0, m0, 0x1000\n"
    "s_mov_b32 s51, s53\n"
    "buffer_load_dwordx4 v16, s[20:23], s51 offen lds\n"
    "s_add_u32 m0, m0, 0x1000\n"
    "s_add_u32 s51, s51, 82944\n"
    "buffer_load_dwordx4 v16, s[20:23], s51 offen lds\n"
    "s_add_u32 m0, m0, 0x1000\n"
    "s_add_u32 s51, s51, 82944\n"
    "buffer_load_dwordx4 v16, s[20:23], s51 offen lds\n"
    "s_add_u32 m0, m0, 0x1000\n"
    "s_add_u32 s51, s51, 82944\n"
    "buffer_load_dwordx4 v16, s[20:23], s51 offen lds\n"
    "s_waitcnt vmcnt(4)\n"
    "s_barrier\n"
    "s_add_u32 m0, m0, 0x1000\n"
    "s_add_u32 s51, s51, 82944\n"
    "buffer_load_dwordx4 v16, s[20:23], s51 offen lds\n"
    "s_add_u32 m0, m0, 0x1000\n"
    "s_add_u32 s51, s51, 82944\n"
    "buffer_load_dwordx4 v16, s[20:23], s51 offen lds\n"
    "s_add_u32 m0, m0, 0x1000\n"
    "s_add_u32 s51, s51, 82944\n"
    "buffer_load_dwordx4 v16, s[20:23], s51 offen lds\n"
    "s_add_u32 m0, m0, 0x1000\n"
    "s_add_u32 s51, s51, 82944\n"
    "buffer_load_dwordx4 v16, s[20:23], s51 offen lds\n"
    "s_add_u32 m0, m0, 0x1000\n"
    "s_add_u32 s51, s51, 82944\n"
    "buffer_load_dwordx4 v16, s[20:23], s51 offen lds\n"
    "s_add_u32 m0, m0, 0x1000\n"
    "s_add_u32 s51, s51, 82944\n"
    "buffer_load_dwordx4 v16, s[20:23], s51 offen lds\n"
    "s_add_u32 m0, m0, 0x1000\n"
    "s_add_u32 s51, s51, 82944\n"
    "buffer_load_dwordx4 v16, s[20:23], s51 offen lds\n"
    "s_add_u32 m0, s52, 50816\n"
    "s_add_u32 s51, s51, 82944\n"
    "buffer_load_dwordx4 v16, s[20:23], s51 offen lds\n"
    "s_waitcnt vmcnt(8)\n"
    "s_barrier\n"
    "s_add_u32 m0, m0, 0x1000\n"
    "s_add_u32 s51, s51, 82944\n"
    "buffer_load_dwordx4 v16, s[20:23], s51 offen lds\n"
    "s_add_u32 m0, m0, 0x1000\n"
    "s_add_u32 s51, s51, 82944\n"
    "buffer_load_dwordx4 v16, s[20:23], s51 offen lds\n"
    "s_add_u32 m0, m0, 0x1000\n"
    "s_add_u32 s51, s51, 82944\n"
    "buffer_load_dwordx4 v16, s[20:23], s51 offen lds\n"
    "s_add_u32 m0, m0, 0x1000\n"
    "s_nop 0\n"
    "buffer_load_dwordx4 v17, s[20:23], 0 offen lds\n"
    "s_add_u32 m0, m0, 0x1000\n"
    "v_add_u32 v17, 331776, v17\n"
    "buffer_load_dwordx4 v17, s[20:23], 0 offen lds\n"
    "s_add_u32 m0, m0, 0x1000\n"
    "v_add_u32 v17, 331776, v17\n"
    "buffer_load_dwordx4 v17, s[20:23], 0 offen lds\n"
    "s_add_u32 m0, m0, 0x1000\n"
    "v_add_u32 v17, 331776, v17\n"
    "buffer_load_dwordx4 v17, s[20:23], 0 offen lds\n"
    "s_waitcnt vmcnt(8)\n"
    "s_barrier\n"
    "s_sleep 3\n"
    "s_lshl_b32 s4, s50, 12\n"
    "s_add_u32 m0, s4, 83584\n"
    "s_nop 0\n"
    "buffer_load_dwordx4 v18, s[24:27], 0 offen offset:0 lds\n"
    "buffer_load_dwordx4 v18, s[24:27], 0 offen offset:1024 lds\n"
    "buffer_load_dwordx4 v18, s[24:27], 0 offen offset:2048 lds\n"
    "buffer_load_dwordx4 v18, s[24:27], 0 offen offset:3072 lds\n"
    "s_add_u32 m0, m0, 0x4000\n"
    "v_add_u32 v18, 0x4000, v18\n"
    "s_nop 0\n"
    "buffer_load_dwordx4 v18, s[24:27], 0 offen offset:0 lds\n"
    "buffer_load_dwordx4 v18, s[24:27], 0 offen offset:1024 lds\n"
    "buffer_load_dwordx4 v18, s[24:27], 0 offen offset:2048 lds\n"
    "buffer_load_dwordx4 v18, s[24:27], 0 offen offset:3072 lds\n"
    "s_add_u32 m0, m0, 0x4000\n"
    "v_add_u32 v18, 0x4000, v18\n"
    "s_lshl_b32 s4, s50, 10\n"
    "s_sub_u32 m0, m0, s4\n"
    "v_subrev_u32 v18, s4, v18\n"
    "buffer_load_dwordx4 v18, s[24:27], 0 offen offset:0 lds\n"
    "buffer_load_dwordx4 v18, s[24:27], 0 offen offset:1024 lds\n"
    "buffer_load_dwordx4 v18, s[24:27], 0 offen offset:2048 lds\n"
    "s_waitcnt vmcnt(11)\n"
    "s_barrier\n"
    "s_waitcnt vmcnt(0)\n"
    "s_branch .Lwn_ph2\n"
    ".Lwn_p2:\n"
    "s_mul_i32 s12, s6, 9\n"
    "s_add_u32 s12, s12, s8\n"
    "s_mul_i32 s14, s9, 18\n"
    "s_add_u32 s54, s14, s12\n"
    "s_mul_i32 s14, s54, 18432\n"
    "s_mov_b32 s34, 4712960\n"
    "s_mov_b32 s35, 0x20000\n"
    "s_mov_b32 s30, 576000\n"
    "s_mov_b32 s31, 0x20000\n"
    "v_and_b32 v32, 15, v28\n"
    "v_lshrrev_b32 v33, 4, v28\n"
    "s_lshl_b32 s4, s13, 4\n"
    "v_add_u32 v34, s4, v32\n"
    "v_mul_u32_u24 v35, 1821, v34\n"
    "v_lshrrev_b32 v35, 16, v35\n"
    "v_lshrrev_b32 v36, 5, v28\n"
    "v_and_b32 v37, 1, v33\n"
    "v_lshl_add_u32 v1, v34, 4, s14\n"
    "s_movk_i32 s4, 1728\n"
    "v_mad_u32_u24 v1, v35, s4, v1\n"
    "s_movk_i32 s4, 9216\n"
    "v_mad_u32_u24 v1, v36, s4, v1\n"
    "s_movk_i32 s4, 1152\n"
    "v_mad_u32_u24 v1, v37, s4, v1\n"
    "v_bfe_u32 v38, v28, 2, 2\n"
    "v_lshl_add_u32 v38, v33, 3, v38\n"
    "v_and_b32 v39, 3, v28\n"
    "s_lshl_b32 s55, s13, 5\n"
    "s_add_u32 s4, s55, 83584\n"
    "v_lshl_add_u32 v2, v39, 3, s4\n"
    "s_movk_i32 s56, 288\n"
    "v_mad_u32_u24 v2, v38, s56, v2\n"
    "v_mul_u32_u24 v3, 1152, v33\n"
    "v_sub_u32 v3, v2, v3\n"
    "v_add_u32 v3, 128*288, v3\n"
    "v_lshlrev_b32 v4, 3, v34\n"
    "v_lshl_add_u32 v4, v37, 2, v4\n"
    "s_movk_i32 s4, 320\n"
    "v_mad_u32_u24 v4, v35, s4, v4\n"
    "s_movk_i32 s4, 304\n"
    "v_mad_u32_u24 v4, v36, s4, v4\n"
    "v_add_u32 v4, 161408, v4\n"
    "v_and_b32 v5, 7, v32\n"
    "v_mul_u32_u24 v5, s4, v5\n"
    "v_add_u32 v5, 161408, v5\n"
    "v_lshl_add_u32 v6, v33, 3, v5\n"
    "v_add_u32 v6, 0x100, v6\n"
    "v_lshl_add_u32 v5, v33, 4, v5\n"
    "v_lshrrev_b32 v40, 1, v32\n"
    "v_and_b32 v41, 1, v32\n"
    "v_mad_u32_u24 v29, v40, 18, v41\n"
    "s_lshr_b32 s4, s12, 1\n"
    "s_mul_i32 s4, s4, 18\n"
    "s_and_b32 s57, s12, 1\n"
    "s_add_u32 s4, s4, s57\n"
    "s_mul_i32 s57, s12, 72\n"
    "s_sub_u32 s57, s57, 16*72-16\n"
    "s_cmp_lt_u32 s12, 16\n"
    "s_cselect_b32 s4, s4, s57\n"
    "s_cselect_b64 vcc, -1, 0\n"
    "s_add_u32 s4, s4, s11\n"
    "v_lshlrev_b32 v42, 1, v32\n"
    "v_cndmask_b32 v42, v29, v42, vcc\n"
    "v_add_u32 v42, s4, v42\n"
    "s_add_u32 s4, s55, s46\n"
    "v_lshl_add_u32 v7, v33, 3, s4\n"
    "v_mad_u32_u24 v7, v42, s56, v7\n"
    "v_cmp_gt_u32 vcc, 1000, v42\n"
    "s_and_b32 vcc_lo, vcc_lo, 0x00ff00ff\n"
    "s_and_b32 vcc_hi, vcc_hi, 0x00ff00ff\n"
    "v_bfrev_b32 v43, 1\n"
    "v_cndmask_b32 v7, v43, v7, vcc\n"
    "v_lshlrev_b32 v41, 4, v41\n"
    "v_mov_b32 v43, 0x3f80\n"
    "v_lshlrev_b32 v43, v41, v43\n"
    "s_mov_b32 vcc_lo, 0x00030003\n"
    "s_mov_b32 vcc_hi, 0x00030003\n"
    "v_cndmask_b32 v8, 0, v43, vcc\n"
    "s_lshl_b64 vcc, vcc, 2\n"
    "v_cndmask_b32 v12, 0, v43, vcc\n"
    "s_lshl_b64 vcc, vcc, 2\n"
    "v_cndmask_b32 v9, 0, v43, vcc\n"
    "s_lshl_b64 vcc, vcc, 2\n"
    "v_cndmask_b32 v13, 0, v43, vcc\n"
    "s_lshl_b64 vcc, vcc, 2\n"
    "v_cndmask_b32 v10, 0, v43, vcc\n"
    "s_lshl_b64 vcc, vcc, 2\n"
    "v_cndmask_b32 v14, 0, v43, vcc\n"
    "s_lshl_b64 vcc, vcc, 2\n"
    "v_cndmask_b32 v11, 0, v43, vcc\n"
    "s_lshl_b64 vcc, vcc, 2\n"
    "v_cndmask_b32 v15, 0, v43, vcc\n"
    "s_setpc_b64 s[44:45]\n"
    ".Lwn_ctl:\n"
    "v_mul_u32_u24 v80, 52, v28\n"
    "v_lshrrev_b32 v80, 8, v80\n"
    "v_mul_u32_u24 v81, 5, v80\n"
    "v_sub_u32 v81, v28, v81\n"
    "s_lshl_b32 s4, s5, 9\n"
    "v_lshl_add_u32 v81, v81, 7, s4\n"
    "v_mul_u32_u24 v80, 82944, v80\n"
    "s_mul_i32 s4, s3, 1152\n"
    "s_add_u32 s48, s4, s51\n"
    "s_add_u32 s48, s48, 331776\n"
    "v_add3_u32 v82, v80, v81, s48\n"
    "s_mul_i32 s53, s6, 1290240\n"
    "s_add_u32 s53, s53, 36864\n"
    "s_add_u32 s4, s4, s53\n"
    "v_add3_u32 v83, v80, v81, s4\n"
    "v_add_u32 v84, 995328, v83\n"
    "s_lshl_b32 s4, s6, 4\n"
    "s_lshr_b32 s48, s3, 3\n"
    "s_add_u32 s4, s4, s48\n"
    "s_mul_i32 s4, s4, 72\n"
    "s_and_b32 s48, s3, 7\n"
    "s_add_u32 s4, s4, s48\n"
    "s_add_u32 s4, s4, 64\n"
    "s_mul_i32 s4, s4, 1152\n"
    "v_lshl_add_u32 v85, v80, 2, s4\n"
    "v_add_u32 v85, v85, v81\n"
    "s_cmp_lt_u32 s7, 63\n"
    "s_cbranch_scc1 .Lwn_ctl2\n"
    "s_bfm_b64 exec, 60, 0\n"
    "buffer_load_dword v27, v82, s[20:23], 0 offen\n"
    "s_mov_b64 exec, -1\n"
    "s_bfm_b64 exec, 60, 0\n"
    "buffer_load_dword v27, v83, s[20:23], 0 offen\n"
    "s_mov_b64 exec, -1\n"
    "s_bfm_b64 exec, 15, 0\n"
    "buffer_load_dword v27, v84, s[20:23], 0 offen\n"
    "s_mov_b64 exec, -1\n"
    "s_bfm_b64 exec, 20, 0\n"
    "buffer_load_dword v27, v85, s[20:23], 0 offen\n"
    "s_mov_b64 exec, -1\n"
    "s_branch .Lwn_exit\n"
    ".Lwn_ctl2:\n"
    "s_lshl_b32 s58, s9, 8\n"
    "s_add_u32 s58, s58, 4709376\n"
    "s_mov_b32 s34, 4712960\n"
    "s_mov_b32 s35, 0x20000\n"
    "buffer_load_dword v30, off, s[32:35], s58 sc1\n"
    "s_mul_i32 s12, s6, 9\n"
    "s_add_u32 s12, s12, s8\n"
    "s_mul_i32 s14, s9, 18\n"
    "s_add_u32 s54, s14, s12\n"
    "s_mul_i32 s14, s54, 18432\n"
    "s_mov_b32 s30, 576000\n"
    "s_mov_b32 s31, 0x20000\n"
    "v_mov_b32 v72, -1\n"
    "v_mov_b32 v73, -1\n"
    "v_mov_b32 v74, -1\n"
    "v_mov_b32 v75, -1\n"
    "s_mul_i32 s4, s9, 18*18432\n"
    "s_mul_i32 s48, s6, 9216\n"
    "s_add_u32 s4, s4, s48\n"
    "s_lshl_b32 s48, s8, 8\n"
    "s_add_u32 s4, s4, s48\n"
    "v_lshl_add_u32 v31, v28, 4, s4\n"
    "v_lshrrev_b32 v78, 4, v28\n"
    "v_lshl_add_u32 v31, v78, 11, v31\n"
    "s_lshl_b32 s59, s54, 8\n"
    "s_add_u32 s59, s59, 4644864\n"
    "s_mul_i32 s4, s9, 18*256\n"
    "s_add_u32 s4, s4, 4644864\n"
    "v_min_u32 v76, 17, v28\n"
    "v_lshl_add_u32 v76, v76, 8, s4\n"
    "s_call_b64 s[44:45], .Lwn_p2\n"
    "s_bfm_b64 exec, 60, 0\n"
    "buffer_load_dword v27, v82, s[20:23], 0 offen\n"
    "s_mov_b64 exec, -1\n"
    "s_bfm_b64 exec, 60, 0\n"
    "buffer_load_dword v27, v83, s[20:23], 0 offen\n"
    "s_mov_b64 exec, -1\n"
    "s_bfm_b64 exec, 15, 0\n"
    "buffer_load_dword v27, v84, s[20:23], 0 offen\n"
    "s_mov_b64 exec, -1\n"
    "s_bfm_b64 exec, 20, 0\n"
    "buffer_load_dword v27, v85, s[20:23], 0 offen\n"
    "s_mov_b64 exec, -1\n"
    "s_barrier\n"
    "s_barrier\n"
    "s_barrier\n"
    "s_barrier\n"
    "s_sleep 6\n"
    "buffer_store_dwordx4 v[72:75], v31, s[32:35], 0 offen sc1\n"
    "v_add_u32 v31, 18432, v31\n"
    "buffer_store_dwordx4 v[72:75], v31, s[32:35], 0 offen sc1\n"
    "v_add_u32 v31, 18432, v31\n"
    "buffer_store_dwordx4 v[72:75], v31, s[32:35], 0 offen sc1\n"
    "v_add_u32 v31, 18432, v31\n"
    "buffer_store_dwordx4 v[72:75], v31, s[32:35], 0 offen sc1\n"
    "v_add_u32 v31, 18432, v31\n"
    "buffer_store_dwordx4 v[72:75], v31, s[32:35], 0 offen sc1\n"
    "v_add_u32 v31, 18432, v31\n"
    "buffer_store_dwordx4 v[72:75], v31, s[32:35], 0 offen sc1\n"
    "v_add_u32 v31, 18432, v31\n"
    "buffer_store_dwordx4 v[72:75], v31, s[32:35], 0 offen sc1\n"
    "v_add_u32 v31, 18432, v31\n"
    "buffer_store_dwordx4 v[72:75], v31, s[32:35], 0 offen sc1\n"
    "v_add_u32 v31, 18432, v31\n"
    "buffer_store_dwordx4 v[72:75], v31, s[32:35], 0 offen sc1\n"
    "v_add_u32 v31, 18432, v31\n"
    "buffer_store_dwordx4 v[72:75], v31, s[32:35], 0 offen sc1\n"
    "v_add_u32 v31, 18432, v31\n"
    "buffer_store_dwordx4 v[72:75], v31, s[32:35], 0 offen sc1\n"
    "v_add_u32 v31, 18432, v31\n"
    "buffer_store_dwordx4 v[72:75], v31, s[32:35], 0 offen sc1\n"
    "v_add_u32 v31, 18432, v31\n"
    "buffer_store_dwordx4 v[72:75], v31, s[32:35], 0 offen sc1\n"
    "v_add_u32 v31, 18432, v31\n"
    "buffer_store_dwordx4 v[72:75], v31, s[32:35], 0 offen sc1\n"
    "v_add_u32 v31, 18432, v31\n"
    "buffer_store_dwordx4 v[72:75], v31, s[32:35], 0 offen sc1\n"
    "v_add_u32 v31, 18432, v31\n"
    "buffer_store_dwordx4 v[72:75], v31, s[32:35], 0 offen sc1\n"
    "v_add_u32 v31, 18432, v31\n"
    "buffer_store_dwordx4 v[72:75], v31, s[32:35], 0 offen sc1\n"
    "v_add_u32 v31, 18432, v31\n"
    "buffer_store_dwordx4 v[72:75], v31, s[32:35], 0 offen sc1\n"
    "v_add_u32 v31, 18432, v31\n"
    "s_barrier\n"
    "s_barrier\n"
    "s_waitcnt vmcnt(0)\n"
    "v_add_u32 v30, 1, v30\n"
    "s_mov_b64 exec, 1\n"
    "buffer_store_dword v30, off, s[32:35], s59 sc1\n"
    "s_mov_b64 exec, -1\n"
    "s_barrier\n"
    "buffer_load_dword v77, v76, s[32:35], 0 offen sc1\n"
    "s_mov_b32 s4, 0x40000\n"
    ".Lwn_fl:\n"
    "s_waitcnt vmcnt(0)\n"
    "v_cmp_ne_u32 vcc, v77, v30\n"
    "s_cbranch_vccz .Lwn_fl_ok\n"
    "s_sub_u32 s4, s4, 1\n"
    "s_cbranch_scc1 .Lwn_fl_ok\n"
    "s_sleep 2\n"
    "buffer_load_dword v77, v76, s[32:35], 0 offen sc1\n"
    "s_branch .Lwn_fl\n"
    ".Lwn_fl_ok:\n"
    "s_branch .Lwn_ph2\n"
    ".Lwn_exit:\n"
    "s_waitcnt vmcnt(0)\n"
    "s_endpgm\n"
    ".Lwn_end:\n"
    :: "{s[0:1]}"(ka), "{s2}"(gb), "{v0}"(tid)
    : "memory", "vcc", "scc", "v1", "v2", "v3", "v4", "v5", "v6", "v7", "v8", "v9", "v10", "v11", "v12", "v13", "v14", "v15", "v16", "v17", "v18", "v19", "v20", "v21", "v22", "v23", "v24", "v25", "v26", "v27", "v28", "v29", "v30", "v31", "v32", "v33", "v34", "v35", "v36", "v37", "v38", "v39", "v40", "v41", "v42", "v43", "v44", "v45", "v46", "v47", "v48", "v49", "v50", "v51", "v52", "v53", "v54", "v55", "v56", "v57", "v58", "v59", "v60", "v61", "v62", "v63", "v64", "v65", "v66", "v67", "v68", "v69", "v70", "v71", "v72", "v73", "v74", "v75", "v76", "v77", "v78", "v79", "v80", "v81", "v82", "v83", "v84", "v85", "v86", "v87", "v88", "v89", "v90", "v91", "v92", "v93", "v94", "v95", "v96", "v97", "v98", "v99", "v100", "v101", "v102", "v103", "v104", "v105", "v106", "v107", "v108", "v109", "v110", "v111", "v112", "v113", "v114", "v115", "v116", "v117", "v118", "v119", "v120", "v121", "v122", "v123", "v124", "v125", "v126", "v127", "v128", "v129", "v130", "v131", "v132", "v133", "v134", "v135", "v136", "v137", "v138", "v139", "v140", "v141", "v142", "v143", "v144", "v145", "v146", "v147", "v148", "v149", "v150", "v151", "v152", "v153", "v154", "v155", "v156", "v157", "v158", "v159", "v160", "v161", "v162", "v163", "v164", "v165", "v166", "v167", "s3", "s4", "s5", "s6", "s7", "s8", "s9", "s10", "s11", "s12", "s13", "s14", "s15", "s16", "s17", "s18", "s19", "s20", "s21", "s22", "s23", "s24", "s25", "s26", "s27", "s28", "s29", "s30", "s31", "s33", "s34", "s35", "s36", "s37", "s38", "s39", "s40", "s41", "s42", "s43", "s44", "s45", "s46", "s47", "s48", "s49", "s50", "s51", "s52", "s53", "s54", "s55", "s56", "s57", "s58", "s59");
#endif
}

extern "C" void kernel_launch(void* const* d_in, const int* in_sizes, int n_in,
                              void* d_out, int out_size, void* d_ws, size_t ws_size,
                              hipStream_t stream) {
    (void)in_sizes; (void)n_in; (void)out_size; (void)ws_size;
    (void)hipFuncSetAttribute((const void*)wavenet_kernel, hipFuncAttributeMaxDynamicSharedMemorySize, 163840);
    wavenet_kernel<<<256, 576, 163840, stream>>>((const char*)d_in[0], (const char*)d_in[1], (const char*)d_in[2], (char*)d_out, (char*)d_ws);
}
```
